```python
import torch

def _softmax(x, axis=-1):
    return torch.softmax(x, dim=axis)
def _gelu(x):
    return torch.nn.functional.gelu(x, approximate="tanh")
def _silu(x):
    return torch.nn.functional.silu(x)
def _sigmoid(x):
    return torch.sigmoid(x)
def _layernorm(x, axis=-1, eps=1e-5):
    m = x.mean(dim=axis, keepdim=True)
    v = x.var(dim=axis, keepdim=True, unbiased=False)
    return (x - m) / torch.sqrt(v + eps)
def _scatter_add(tgt, idx, src, axis=0):
    return tgt.clone().index_add_(axis, idx, src)
def _segment_sum(src, seg, n):
    out = torch.zeros((n,) + src.shape[1:], dtype=src.dtype)
    return out.index_add_(0, seg, src)
def _pad(x, pw):
    flat = tuple(v for pair in reversed(pw) for v in pair)
    return torch.nn.functional.pad(x, flat)

kernel_name = 'sequential_preLN__spectral_gfnet__mixer_channel_mlp__prompt'

def setup_inputs():

    _w = (lambda a: (lambda t: t.to(torch.bfloat16) if t.is_floating_point() else t)(torch.as_tensor(a)))
    def _A_setup():
        B, S, D = 1, 2048, 640
        Df = D // 2 + 1
        return {
            "x": _w((lambda *s: torch.randn(*s, dtype=torch.bfloat16) * 0.05)(B, S, D)),
            "w_r": _w((lambda *s: torch.randn(*s, dtype=torch.bfloat16) * 0.8)(S, Df) * 0.02),
            "w_i": _w((lambda *s: torch.randn(*s, dtype=torch.bfloat16) * 0.8)(S, Df) * 0.02),
        }
    _A_in = _A_setup()

    def _B_setup():
        B, S, D, F = 1, 2048, 640, 1728
        return {
            "x": _w((lambda *s: torch.randn(*s, dtype=torch.bfloat16) * 0.05)(B, S, D)),
            "w1": _w((lambda *s: torch.randn(*s, dtype=torch.bfloat16) * 0.8)(D, F) * (D ** -0.5)),
            "b1": _w((lambda *s: torch.randn(*s, dtype=torch.bfloat16) * 0.4)(F)),
            "w2": _w((lambda *s: torch.randn(*s, dtype=torch.bfloat16) * 0.05)(F, D) * (F ** -0.5)),
            "b2": _w((lambda *s: torch.randn(*s, dtype=torch.bfloat16) * 0.05)(D)),
        }
    _B_in = _B_setup()

    return {
        "A_x": _A_in["x"],
        "A_w_r": _A_in["w_r"],
        "A_w_i": _A_in["w_i"],
        "B_x": _B_in["x"],
        "B_w1": _B_in["w1"],
        "B_b1": _B_in["b1"],
        "B_w2": _B_in["w2"],
        "B_b2": _B_in["b2"],
    }

def reference(A_x, A_w_r, A_w_i, B_x, B_w1, B_b1, B_w2, B_b2):
    def _A_ref(x, w_r, w_i):
        B, S, D = x.shape
        X = (lambda x, axes=(-2,-1): torch.fft.rfft2(x.float(), dim=axes))(x, axes=(-2, -1))
        W = w_r + 1j * w_i
        X = X * W
        y = (lambda x, s, axes=(-2,-1): torch.fft.irfft2(x, s=s, dim=axes).to(torch.bfloat16))(X, (S, D), axes=(-2, -1))
        return y

    def _B_ref(x, w1, b1, w2, b2):
        y = _layernorm(x, axis=-1)
        h = _gelu(torch.einsum("bsd,df->bsf", y, w1) + b1)
        return torch.einsum("bsf,fd->bsd", h, w2) + b2

    x = A_x
    h = x + _A_ref(_layernorm(x, axis=-1), A_w_r, A_w_i)
    h2 = h + _B_ref(_layernorm(h, axis=-1), B_w1, B_b1, B_w2, B_b2)
    return h2
```

```cpp
#include <hip/hip_runtime.h>
#include <hip/hip_bf16.h>
#include <stdint.h>

static constexpr int kS  = 2048;
static constexpr int kD  = 640;
static constexpr int kDF = 321;
static constexpr int kF  = 1728;
static constexpr float kLnEps = 1e-5f;

static constexpr size_t OFF_T     = 0;
static constexpr size_t OFF_T2    = OFF_T   + (size_t)kDF * kS * 8;
static constexpr size_t OFF_H     = OFF_T2  + (size_t)kDF * kS * 8;
static constexpr size_t OFF_YLN2  = OFF_H   + (size_t)kS * kD * 2;
static constexpr size_t OFF_G     = OFF_YLN2+ (size_t)kS * kD * 2;
static constexpr size_t OFF_W1T   = OFF_G   + (size_t)kS * kF * 2;
static constexpr size_t OFF_W2T   = OFF_W1T + (size_t)kF * kD * 2;
static constexpr size_t OFF_TW2048= OFF_W2T + (size_t)kD * kF * 2;
static constexpr size_t OFF_TW640 = OFF_TW2048 + (size_t)2048 * 8;

typedef unsigned short bf16_t;

__device__ __forceinline__ float bf2f(bf16_t u) {
    return __uint_as_float(((unsigned)u) << 16);
}
__device__ __forceinline__ bf16_t f2bf(float f) {
    unsigned u = __float_as_uint(f);
    u += 0x7fffu + ((u >> 16) & 1u);
    return (bf16_t)(u >> 16);
}
__device__ __forceinline__ float rbf(float f) { return bf2f(f2bf(f)); }

__device__ __forceinline__ float2 cadd(float2 a, float2 b) { return make_float2(a.x + b.x, a.y + b.y); }
__device__ __forceinline__ float2 csub(float2 a, float2 b) { return make_float2(a.x - b.x, a.y - b.y); }
__device__ __forceinline__ float2 cmul(float2 a, float2 b) {
    return make_float2(__fmaf_rn(a.x, b.x, -a.y * b.y), __fmaf_rn(a.x, b.y, a.y * b.x));
}
__device__ __forceinline__ float2 cmulc(float2 a, float2 b) {
    return make_float2(__fmaf_rn(a.x, b.x, a.y * b.y), __fmaf_rn(a.y, b.x, -a.x * b.y));
}
template <bool INV>
__device__ __forceinline__ float2 cmul_j(float2 a) {
    return INV ? make_float2(-a.y, a.x) : make_float2(a.y, -a.x);
}

template <bool INV>
__device__ __forceinline__ void dft4(float2 &b0, float2 &b1, float2 &b2, float2 &b3) {
    float2 t0 = cadd(b0, b2), t1 = csub(b0, b2), t2 = cadd(b1, b3), t3 = cmul_j<INV>(csub(b1, b3));
    b0 = cadd(t0, t2); b2 = csub(t0, t2); b1 = cadd(t1, t3); b3 = csub(t1, t3);
}
template <bool INV>
__device__ __forceinline__ void dft5(float2 a[5]) {
    const float c1 = 0.30901699437494745f, c2 = -0.8090169943749473f;
    const float s1 = 0.9510565162951535f,  s2 = 0.5877852522924731f;
    float2 b1 = cadd(a[1], a[4]), b4 = csub(a[1], a[4]);
    float2 b2 = cadd(a[2], a[3]), b3 = csub(a[2], a[3]);
    float2 t1 = make_float2(a[0].x + c1 * b1.x + c2 * b2.x, a[0].y + c1 * b1.y + c2 * b2.y);
    float2 t2 = make_float2(a[0].x + c2 * b1.x + c1 * b2.x, a[0].y + c2 * b1.y + c1 * b2.y);
    float2 u1 = make_float2(s1 * b4.x + s2 * b3.x, s1 * b4.y + s2 * b3.y);
    float2 u2 = make_float2(s2 * b4.x - s1 * b3.x, s2 * b4.y - s1 * b3.y);
    float2 iu1 = cmul_j<INV>(u1), iu2 = cmul_j<INV>(u2);
    a[0] = make_float2(a[0].x + b1.x + b2.x, a[0].y + b1.y + b2.y);
    a[1] = cadd(t1, iu1); a[4] = csub(t1, iu1);
    a[2] = cadd(t2, iu2); a[3] = csub(t2, iu2);
}
template <bool INV>
__device__ __forceinline__ void dft8(float2 a[8]) {
    const float r2 = 0.70710678118654752f;
    float2 e0 = a[0], e1 = a[2], e2 = a[4], e3 = a[6];
    float2 o0 = a[1], o1 = a[3], o2 = a[5], o3 = a[7];
    dft4<INV>(e0, e1, e2, e3);
    dft4<INV>(o0, o1, o2, o3);
    float2 p1, p3;
    if (!INV) {
        p1 = make_float2((o1.x + o1.y) * r2, (o1.y - o1.x) * r2);
        p3 = make_float2((o3.y - o3.x) * r2, -(o3.x + o3.y) * r2);
    } else {
        p1 = make_float2((o1.x - o1.y) * r2, (o1.x + o1.y) * r2);
        p3 = make_float2(-(o3.x + o3.y) * r2, (o3.x - o3.y) * r2);
    }
    float2 p2 = cmul_j<INV>(o2);
    a[0] = cadd(e0, o0); a[4] = csub(e0, o0);
    a[1] = cadd(e1, p1); a[5] = csub(e1, p1);
    a[2] = cadd(e2, p2); a[6] = csub(e2, p2);
    a[3] = cadd(e3, p3); a[7] = csub(e3, p3);
}

template <int R, bool INV> struct Dft;
template <bool INV> struct Dft<4, INV> {
    __device__ __forceinline__ static void run(float2 a[4]) { dft4<INV>(a[0], a[1], a[2], a[3]); }
};
template <bool INV> struct Dft<5, INV> {
    __device__ __forceinline__ static void run(float2 a[5]) { dft5<INV>(a); }
};
template <bool INV> struct Dft<8, INV> {
    __device__ __forceinline__ static void run(float2 a[8]) { dft8<INV>(a); }
};

template <int R, int NT, int NN, int SS, int NROWS, int TWSCALE, bool INV>
__device__ __forceinline__ void stockham_pass(const float2* __restrict__ in, float2* __restrict__ out,
                                              const float2* __restrict__ tw, int tid, int nthreads) {
    constexpr int M = NN / R;
    constexpr int NB = NT / R;
    for (int bt = tid; bt < NROWS * NB; bt += nthreads) {
        const int r   = bt / NB;
        const int rem = bt - r * NB;
        const int p   = rem / SS;
        const int q   = rem - p * SS;
        const float2* rin = in + r * NT;
        float2* rout = out + r * NT;
        float2 a[R];
#pragma unroll
        for (int k = 0; k < R; ++k) a[k] = rin[SS * (p + k * M) + q];
        Dft<R, INV>::run(a);
        if (NN != R) {
#pragma unroll
            for (int k = 1; k < R; ++k) {
                float2 w = tw[p * k * TWSCALE];
                a[k] = INV ? cmulc(a[k], w) : cmul(a[k], w);
            }
        }
#pragma unroll
        for (int k = 0; k < R; ++k) rout[SS * (R * p + k) + q] = a[k];
    }
}

__global__ void __launch_bounds__(256) prep_kernel(const bf16_t* __restrict__ W1, const bf16_t* __restrict__ W2,
                                                   bf16_t* __restrict__ W1t, bf16_t* __restrict__ W2t,
                                                   float2* __restrict__ tw2048, float2* __restrict__ tw640) {
    const int nt1 = (kD / 64) * (kF / 64);
    const int nt2 = (kF / 64) * (kD / 64);
    const int tid = threadIdx.x;
    if (blockIdx.x < nt1 + nt2) {
        const bf16_t* src; bf16_t* dst; int R, C, tr, tc;
        if (blockIdx.x < (unsigned)nt1) {
            src = W1; dst = W1t; R = kD; C = kF;
            tr = blockIdx.x / (kF / 64); tc = blockIdx.x % (kF / 64);
        } else {
            src = W2; dst = W2t; R = kF; C = kD;
            int b = blockIdx.x - nt1;
            tr = b / (kD / 64); tc = b % (kD / 64);
        }
        (void)R;
        __shared__ bf16_t tile[64][66];
        const int r0 = tr * 64, c0 = tc * 64;
#pragma unroll
        for (int i = 0; i < 2; ++i) {
            int u = tid + 256 * i;
            int row = u >> 3, ch = u & 7;
            uint4 v = *reinterpret_cast<const uint4*>(src + (size_t)(r0 + row) * C + c0 + 8 * ch);
            const bf16_t* e = reinterpret_cast<const bf16_t*>(&v);
#pragma unroll
            for (int j = 0; j < 8; ++j) tile[row][8 * ch + j] = e[j];
        }
        __syncthreads();
#pragma unroll
        for (int i = 0; i < 2; ++i) {
            int u = tid + 256 * i;
            int orow = u >> 3, ch = u & 7;
            uint4 v;
            bf16_t* e = reinterpret_cast<bf16_t*>(&v);
#pragma unroll
            for (int j = 0; j < 8; ++j) e[j] = tile[8 * ch + j][orow];
            *reinterpret_cast<uint4*>(dst + (size_t)(c0 + orow) * R + r0 + 8 * ch) = v;
        }
    } else {
        for (int j = tid; j < 2048; j += 256) {
            float s, c;
            sincospif(-2.0f * (float)j / 2048.0f, &s, &c);
            tw2048[j] = make_float2(c, s);
        }
        for (int j = tid; j < 640; j += 256) {
            float s, c;
            sincospif(-2.0f * (float)j / 640.0f, &s, &c);
            tw640[j] = make_float2(c, s);
        }
    }
}

static constexpr int kRowsWG = 8;

__global__ void __launch_bounds__(256) ln_rfft_kernel(const bf16_t* __restrict__ x,
                                                      const float2* __restrict__ tw640_g,
                                                      float2* __restrict__ T) {
    __shared__ float2 tw[640];
    __shared__ float2 buf[2][kRowsWG * 320];
    const int tid = threadIdx.x;
    const int lane = tid & 63, wave = tid >> 6;
    const int row0 = blockIdx.x * kRowsWG;
    for (int i = tid; i < 640; i += 256) tw[i] = tw640_g[i];

    {
        float* bufr = reinterpret_cast<float*>(buf[0]);
#pragma unroll
        for (int rr = 0; rr < 2; ++rr) {
            const int r = 2 * wave + rr;
            const bf16_t* xr = x + (size_t)(row0 + r) * kD;
            float v[10];
            float sum = 0.f;
#pragma unroll
            for (int i = 0; i < 10; ++i) { v[i] = bf2f(xr[lane + 64 * i]); sum += v[i]; }
#pragma unroll
            for (int off = 32; off > 0; off >>= 1) sum += __shfl_xor(sum, off);
            const float mean = sum * (1.0f / kD);
            const float mb = rbf(mean);
            float ss = 0.f;
#pragma unroll
            for (int i = 0; i < 10; ++i) { float d = v[i] - mean; ss += d * d; }
#pragma unroll
            for (int off = 32; off > 0; off >>= 1) ss += __shfl_xor(ss, off);
            const float var = ss * (1.0f / kD);
            const float vb = rbf(var);
            const float den = rbf(sqrtf(rbf(vb + kLnEps)));
#pragma unroll
            for (int i = 0; i < 10; ++i) {
                float y = rbf(rbf(v[i] - mb) / den);
                bufr[r * kD + lane + 64 * i] = y;
            }
        }
    }
    __syncthreads();
    stockham_pass<8, 320, 320, 1, kRowsWG, 2, false>(buf[0], buf[1], tw, tid, 256);
    __syncthreads();
    stockham_pass<8, 320, 40, 8, kRowsWG, 16, false>(buf[1], buf[0], tw, tid, 256);
    __syncthreads();
    stockham_pass<5, 320, 5, 64, kRowsWG, 128, false>(buf[0], buf[1], tw, tid, 256);
    __syncthreads();
    const float2* Z = buf[1];
    for (int k = tid; k < kDF; k += 256) {
        const int km = (320 - k) % 320;
        const float2 w = tw[k];
        float2 outv[kRowsWG];
#pragma unroll
        for (int r = 0; r < kRowsWG; ++r) {
            const float2 Ze = Z[r * 320 + (k % 320)];
            const float2 Zm = Z[r * 320 + km];
            const float2 E = make_float2(0.5f * (Ze.x + Zm.x), 0.5f * (Ze.y - Zm.y));
            const float dx = Ze.x - Zm.x, dy = Ze.y + Zm.y;
            const float2 O = make_float2(0.5f * dy, -0.5f * dx);
            outv[r] = cadd(E, cmul(w, O));
        }
        float4* dst = reinterpret_cast<float4*>(T + (size_t)k * kS + row0);
#pragma unroll
        for (int j = 0; j < kRowsWG / 2; ++j)
            dst[j] = make_float4(outv[2 * j].x, outv[2 * j].y, outv[2 * j + 1].x, outv[2 * j + 1].y);
    }
}

__global__ void __launch_bounds__(256) colfft_kernel(const float2* __restrict__ T,
                                                     float2* __restrict__ T2,
                                                     const bf16_t* __restrict__ w_r,
                                                     const bf16_t* __restrict__ w_i,
                                                     const float2* __restrict__ tw2048_g) {
    __shared__ float2 tw[2048];
    __shared__ float2 buf[2][2048];
    const int tid = threadIdx.x;
    const int l = blockIdx.x;
    float wvr[8], wvi[8];
#pragma unroll
    for (int j = 0; j < 8; ++j) {
        const int s = tid + 256 * j;
        wvr[j] = bf2f(w_r[(size_t)s * kDF + l]);
        wvi[j] = bf2f(w_i[(size_t)s * kDF + l]);
    }
    const float4* tw4 = reinterpret_cast<const float4*>(tw2048_g);
    float4* twd = reinterpret_cast<float4*>(tw);
#pragma unroll
    for (int i = 0; i < 4; ++i) twd[tid + 256 * i] = tw4[tid + 256 * i];
    const float4* col4 = reinterpret_cast<const float4*>(T + (size_t)l * kS);
    float4* b04 = reinterpret_cast<float4*>(buf[0]);
#pragma unroll
    for (int i = 0; i < 4; ++i) b04[tid + 256 * i] = col4[tid + 256 * i];
    __syncthreads();
    stockham_pass<8, 2048, 2048, 1, 1, 1, false>(buf[0], buf[1], tw, tid, 256);
    __syncthreads();
    stockham_pass<8, 2048, 256, 8, 1, 8, false>(buf[1], buf[0], tw, tid, 256);
    __syncthreads();
    stockham_pass<8, 2048, 32, 64, 1, 64, false>(buf[0], buf[1], tw, tid, 256);
    __syncthreads();
    {
        const float sc = 1.0f / 2048.0f;
#pragma unroll
        for (int t2 = 0; t2 < 2; ++t2) {
            const int q = tid + 256 * t2;
            float2 a[4];
#pragma unroll
            for (int k = 0; k < 4; ++k) a[k] = buf[1][512 * k + q];
            dft4<false>(a[0], a[1], a[2], a[3]);
#pragma unroll
            for (int k = 0; k < 4; ++k) {
                const int j = 2 * k + t2;
                const float2 wc = make_float2(wvr[j] * sc, wvi[j] * sc);
                buf[0][512 * k + q] = cmul(a[k], wc);
            }
        }
    }
    __syncthreads();
    stockham_pass<8, 2048, 2048, 1, 1, 1, true>(buf[0], buf[1], tw, tid, 256);
    __syncthreads();
    stockham_pass<8, 2048, 256, 8, 1, 8, true>(buf[1], buf[0], tw, tid, 256);
    __syncthreads();
    stockham_pass<8, 2048, 32, 64, 1, 64, true>(buf[0], buf[1], tw, tid, 256);
    __syncthreads();
#pragma unroll
    for (int t2 = 0; t2 < 2; ++t2) {
        const int q = tid + 256 * t2;
        float2 a[4];
#pragma unroll
        for (int k = 0; k < 4; ++k) a[k] = buf[1][512 * k + q];
        dft4<true>(a[0], a[1], a[2], a[3]);
#pragma unroll
        for (int k = 0; k < 4; ++k) T2[(size_t)l * kS + 512 * k + q] = a[k];
    }
}

__global__ void __launch_bounds__(256) irfft_ln_kernel(const bf16_t* __restrict__ x,
                                                       const float2* __restrict__ T2,
                                                       const float2* __restrict__ tw640_g,
                                                       bf16_t* __restrict__ hout,
                                                       bf16_t* __restrict__ yln2) {
    __shared__ float2 tw[640];
    __shared__ float2 X[kRowsWG][322];
    __shared__ float2 buf[2][kRowsWG * 320];
    const int tid = threadIdx.x;
    const int row0 = blockIdx.x * kRowsWG;
    for (int i = tid; i < 640; i += 256) tw[i] = tw640_g[i];
    for (int k = tid; k < kDF; k += 256) {
        const float4* src = reinterpret_cast<const float4*>(T2 + (size_t)k * kS + row0);
        float4 v0 = src[0], v1 = src[1], v2 = src[2], v3 = src[3];
        X[0][k] = make_float2(v0.x, v0.y); X[1][k] = make_float2(v0.z, v0.w);
        X[2][k] = make_float2(v1.x, v1.y); X[3][k] = make_float2(v1.z, v1.w);
        X[4][k] = make_float2(v2.x, v2.y); X[5][k] = make_float2(v2.z, v2.w);
        X[6][k] = make_float2(v3.x, v3.y); X[7][k] = make_float2(v3.z, v3.w);
    }
    __syncthreads();
    for (int task = tid; task < kRowsWG * 320; task += 256) {
        const int r = task / 320, k = task - r * 320;
        float2 Xk = X[r][k];
        float2 Xm = X[r][320 - k];
        if (k == 0) { Xk.y = 0.f; Xm.y = 0.f; }
        const float2 E = make_float2(0.5f * (Xk.x + Xm.x), 0.5f * (Xk.y - Xm.y));
        const float2 d = make_float2(Xk.x - Xm.x, Xk.y + Xm.y);
        float2 O = cmulc(d, tw[k]);
        O.x *= 0.5f; O.y *= 0.5f;
        buf[0][r * 320 + k] = make_float2(E.x - O.y, E.y + O.x);
    }
    __syncthreads();
    stockham_pass<8, 320, 320, 1, kRowsWG, 2, true>(buf[0], buf[1], tw, tid, 256);
    __syncthreads();
    stockham_pass<8, 320, 40, 8, kRowsWG, 16, true>(buf[1], buf[0], tw, tid, 256);
    __syncthreads();
    stockham_pass<5, 320, 5, 64, kRowsWG, 128, true>(buf[0], buf[1], tw, tid, 256);
    __syncthreads();
    const int r = tid >> 5;
    const int l32 = tid & 31;
    const float* zr = reinterpret_cast<const float*>(buf[1] + r * 320);
    const bf16_t* xrow = x + (size_t)(row0 + r) * kD;
    bf16_t* hrow = hout + (size_t)(row0 + r) * kD;
    bf16_t* yrow = yln2 + (size_t)(row0 + r) * kD;
    float hv[20];
    float sum = 0.f;
#pragma unroll
    for (int i = 0; i < 20; ++i) {
        const int e = l32 + 32 * i;
        const float yv = zr[e] * (1.0f / 320.0f);
        const float hf = rbf(bf2f(xrow[e]) + rbf(yv));
        hv[i] = hf;
        hrow[e] = f2bf(hf);
        sum += hf;
    }
#pragma unroll
    for (int off = 16; off > 0; off >>= 1) sum += __shfl_xor(sum, off);
    const float mean = sum * (1.0f / kD);
    const float mb = rbf(mean);
    float ss = 0.f;
#pragma unroll
    for (int i = 0; i < 20; ++i) { float d = hv[i] - mean; ss += d * d; }
#pragma unroll
    for (int off = 16; off > 0; off >>= 1) ss += __shfl_xor(ss, off);
    const float var = ss * (1.0f / kD);
    const float den = rbf(sqrtf(rbf(rbf(var) + kLnEps)));
    float y1[20];
    float sum2 = 0.f;
#pragma unroll
    for (int i = 0; i < 20; ++i) { y1[i] = rbf(rbf(hv[i] - mb) / den); sum2 += y1[i]; }
#pragma unroll
    for (int off = 16; off > 0; off >>= 1) sum2 += __shfl_xor(sum2, off);
    const float mean2 = sum2 * (1.0f / kD);
    const float mb2 = rbf(mean2);
    float ss2 = 0.f;
#pragma unroll
    for (int i = 0; i < 20; ++i) { float d = y1[i] - mean2; ss2 += d * d; }
#pragma unroll
    for (int off = 16; off > 0; off >>= 1) ss2 += __shfl_xor(ss2, off);
    const float var2 = ss2 * (1.0f / kD);
    const float den2 = rbf(sqrtf(rbf(rbf(var2) + kLnEps)));
#pragma unroll
    for (int i = 0; i < 20; ++i) {
        const int e = l32 + 32 * i;
        yrow[e] = f2bf(rbf(y1[i] - mb2) / den2);
    }
}

using bf16x8 = __attribute__((ext_vector_type(8))) __bf16;
using f32x16 = __attribute__((ext_vector_type(16))) float;
typedef unsigned __attribute__((ext_vector_type(4))) u32x4;

template <int TM, int TN, int WM, int WN, int EPI>
__global__ void __launch_bounds__(256) gemm_bt_kernel(const bf16_t* __restrict__ A, int lda,
                                                      const bf16_t* __restrict__ Bt, int ldb, int K,
                                                      const bf16_t* __restrict__ bias,
                                                      const bf16_t* __restrict__ resid,
                                                      bf16_t* __restrict__ C, int ldc) {
    constexpr int BK  = 64;
    constexpr int LDS_STRIDE = BK + 8;
    constexpr int WTM = TM / WM, WTN = TN / WN;
    constexpr int MB  = WTM / 32, NB = WTN / 32;
    constexpr int AU  = TM * BK / 8 / 256;
    constexpr int BU  = TN * BK / 8 / 256;
    static_assert(AU >= 1 && BU >= 1, "tile too small");
    static_assert(AU <= 8 && BU <= 2, "staging unroll fixed");
    __shared__ __attribute__((aligned(16))) bf16_t As[2][TM][LDS_STRIDE];
    __shared__ __attribute__((aligned(16))) bf16_t Bs[2][TN][LDS_STRIDE];

    const int tid = threadIdx.x, lane = tid & 63, wave = tid >> 6;
    const int wm = wave / WN, wn = wave % WN;
    const int m0 = blockIdx.y * TM, n0 = blockIdx.x * TN;

    u32x4 ra0, ra1, ra2, ra3, ra4, ra5, ra6, ra7, rb0, rb1;
    ra1 = ra2 = ra3 = ra4 = ra5 = ra6 = ra7 = rb1 = (u32x4)(0u, 0u, 0u, 0u);

#define A_ADDR(I, K0) (A + (size_t)(m0 + ((tid + 256 * (I)) >> 3)) * lda + (K0) + 8 * ((tid + 256 * (I)) & 7))
#define B_ADDR(I, K0) (Bt + (size_t)(n0 + ((tid + 256 * (I)) >> 3)) * ldb + (K0) + 8 * ((tid + 256 * (I)) & 7))
#define AS_PTR(BUF, I) (&As[(BUF)][(tid + 256 * (I)) >> 3][8 * ((tid + 256 * (I)) & 7)])
#define BS_PTR(BUF, I) (&Bs[(BUF)][(tid + 256 * (I)) >> 3][8 * ((tid + 256 * (I)) & 7)])
#define GLOAD(K0)                                                       \
    do {                                                                \
        ra0 = *reinterpret_cast<const u32x4*>(A_ADDR(0, K0));           \
        if (AU > 1) ra1 = *reinterpret_cast<const u32x4*>(A_ADDR(1, K0)); \
        if (AU > 2) ra2 = *reinterpret_cast<const u32x4*>(A_ADDR(2, K0)); \
        if (AU > 3) ra3 = *reinterpret_cast<const u32x4*>(A_ADDR(3, K0)); \
        if (AU > 4) ra4 = *reinterpret_cast<const u32x4*>(A_ADDR(4, K0)); \
        if (AU > 5) ra5 = *reinterpret_cast<const u32x4*>(A_ADDR(5, K0)); \
        if (AU > 6) ra6 = *reinterpret_cast<const u32x4*>(A_ADDR(6, K0)); \
        if (AU > 7) ra7 = *reinterpret_cast<const u32x4*>(A_ADDR(7, K0)); \
        rb0 = *reinterpret_cast<const u32x4*>(B_ADDR(0, K0));           \
        if (BU > 1) rb1 = *reinterpret_cast<const u32x4*>(B_ADDR(1, K0)); \
    } while (0)
#define SSTORE(BUF)                                                     \
    do {                                                                \
        *reinterpret_cast<u32x4*>(AS_PTR(BUF, 0)) = ra0;                \
        if (AU > 1) *reinterpret_cast<u32x4*>(AS_PTR(BUF, 1)) = ra1;    \
        if (AU > 2) *reinterpret_cast<u32x4*>(AS_PTR(BUF, 2)) = ra2;    \
        if (AU > 3) *reinterpret_cast<u32x4*>(AS_PTR(BUF, 3)) = ra3;    \
        if (AU > 4) *reinterpret_cast<u32x4*>(AS_PTR(BUF, 4)) = ra4;    \
        if (AU > 5) *reinterpret_cast<u32x4*>(AS_PTR(BUF, 5)) = ra5;    \
        if (AU > 6) *reinterpret_cast<u32x4*>(AS_PTR(BUF, 6)) = ra6;    \
        if (AU > 7) *reinterpret_cast<u32x4*>(AS_PTR(BUF, 7)) = ra7;    \
        *reinterpret_cast<u32x4*>(BS_PTR(BUF, 0)) = rb0;                \
        if (BU > 1) *reinterpret_cast<u32x4*>(BS_PTR(BUF, 1)) = rb1;    \
    } while (0)

    f32x16 acc[MB][NB];
#pragma unroll
    for (int i = 0; i < MB; ++i)
#pragma unroll
        for (int j = 0; j < NB; ++j)
#pragma unroll
            for (int e = 0; e < 16; ++e) acc[i][j][e] = 0.f;

    const int KT = K / BK;
    GLOAD(0);
    SSTORE(0);
    __syncthreads();
    for (int kt = 0; kt < KT; ++kt) {
        const int cur = kt & 1;
        if (kt + 1 < KT) GLOAD((kt + 1) * BK);
#pragma unroll
        for (int kk = 0; kk < 4; ++kk) {
            bf16x8 af[MB], bfv[NB];
#pragma unroll
            for (int mi = 0; mi < MB; ++mi)
                af[mi] = *reinterpret_cast<const bf16x8*>(
                    &As[cur][wm * WTM + mi * 32 + (lane & 31)][kk * 16 + (lane >> 5) * 8]);
#pragma unroll
            for (int ni = 0; ni < NB; ++ni)
                bfv[ni] = *reinterpret_cast<const bf16x8*>(
                    &Bs[cur][wn * WTN + ni * 32 + (lane & 31)][kk * 16 + (lane >> 5) * 8]);
#pragma unroll
            for (int mi = 0; mi < MB; ++mi)
#pragma unroll
                for (int ni = 0; ni < NB; ++ni)
                    acc[mi][ni] = __builtin_amdgcn_mfma_f32_32x32x16_bf16(af[mi], bfv[ni], acc[mi][ni], 0, 0, 0);
        }
        if (kt + 1 < KT) SSTORE(cur ^ 1);
        __syncthreads();
    }
#undef GLOAD
#undef SSTORE
#undef A_ADDR
#undef B_ADDR
#undef AS_PTR
#undef BS_PTR
#pragma unroll
    for (int mi = 0; mi < MB; ++mi)
#pragma unroll
        for (int ni = 0; ni < NB; ++ni) {
            const int col = n0 + wn * WTN + ni * 32 + (lane & 31);
            const float bcol = bf2f(bias[col]);
#pragma unroll
            for (int e = 0; e < 16; ++e) {
                const int row = m0 + wm * WTM + mi * 32 + (e & 3) + 8 * (e >> 2) + 4 * (lane >> 5);
                const float accf = rbf(acc[mi][ni][e]);
                const float t = rbf(accf + bcol);
                if (EPI == 0) {
                    const float inner = 0.7978845608028654f * (t + 0.044715f * t * t * t);
                    const float g = 0.5f * t * (1.0f + tanhf(inner));
                    C[(size_t)row * ldc + col] = f2bf(g);
                } else {
                    const float hv = bf2f(resid[(size_t)row * ldc + col]);
                    C[(size_t)row * ldc + col] = f2bf(hv + t);
                }
            }
        }
}

extern "C" void kernel_launch(void* const* d_in, const int* in_sizes, int n_in,
                              void* d_out, int out_size, void* d_ws, size_t ws_size,
                              hipStream_t stream) {
    (void)in_sizes; (void)n_in; (void)out_size; (void)ws_size;
    const bf16_t* A_x  = (const bf16_t*)d_in[0];
    const bf16_t* A_wr = (const bf16_t*)d_in[1];
    const bf16_t* A_wi = (const bf16_t*)d_in[2];
    const bf16_t* B_w1 = (const bf16_t*)d_in[4];
    const bf16_t* B_b1 = (const bf16_t*)d_in[5];
    const bf16_t* B_w2 = (const bf16_t*)d_in[6];
    const bf16_t* B_b2 = (const bf16_t*)d_in[7];
    bf16_t* out = (bf16_t*)d_out;

    char* ws = (char*)d_ws;
    float2* T      = (float2*)(ws + OFF_T);
    float2* T2     = (float2*)(ws + OFF_T2);
    bf16_t* hbuf   = (bf16_t*)(ws + OFF_H);
    bf16_t* yln2   = (bf16_t*)(ws + OFF_YLN2);
    bf16_t* Hbuf   = (bf16_t*)(ws + OFF_G);
    bf16_t* W1t    = (bf16_t*)(ws + OFF_W1T);
    bf16_t* W2t    = (bf16_t*)(ws + OFF_W2T);
    float2* tw2048 = (float2*)(ws + OFF_TW2048);
    float2* tw640  = (float2*)(ws + OFF_TW640);

    prep_kernel<<<541, 256, 0, stream>>>(B_w1, B_w2, W1t, W2t, tw2048, tw640);
    ln_rfft_kernel<<<kS / kRowsWG, 256, 0, stream>>>(A_x, tw640, T);
    colfft_kernel<<<kDF, 256, 0, stream>>>(T, T2, A_wr, A_wi, tw2048);
    irfft_ln_kernel<<<kS / kRowsWG, 256, 0, stream>>>(A_x, T2, tw640, hbuf, yln2);
    gemm_bt_kernel<256, 64, 4, 1, 0><<<dim3(kF / 64, kS / 256), 256, 0, stream>>>(
        yln2, kD, W1t, kD, kD, B_b1, nullptr, Hbuf, kF);
    gemm_bt_kernel<128, 64, 2, 2, 1><<<dim3(kD / 64, kS / 128), 256, 0, stream>>>(
        Hbuf, kF, W2t, kF, kF, B_b2, hbuf, out, kD);
}
```

```cpp
#include <hip/hip_runtime.h>
#include <hip/hip_bf16.h>
#include <stdint.h>

static constexpr int kS  = 2048;
static constexpr int kD  = 640;
static constexpr int kDF = 321;
static constexpr int kF  = 1728;
static constexpr float kLnEps = 1e-5f;

static constexpr size_t OFF_T     = 0;
static constexpr size_t OFF_T2    = OFF_T   + (size_t)kDF * kS * 8;
static constexpr size_t OFF_H     = OFF_T2  + (size_t)kDF * kS * 8;
static constexpr size_t OFF_YLN2  = OFF_H   + (size_t)kS * kD * 2;
static constexpr size_t OFF_G     = OFF_YLN2+ (size_t)kS * kD * 2;
static constexpr size_t OFF_W1T   = OFF_G   + (size_t)kS * kF * 2;
static constexpr size_t OFF_W2T   = OFF_W1T + (size_t)kF * kD * 2;
static constexpr size_t OFF_TW2048= OFF_W2T + (size_t)kD * kF * 2;
static constexpr size_t OFF_TW640 = OFF_TW2048 + (size_t)2048 * 8;
static constexpr size_t OFF_WT    = OFF_TW640 + (size_t)640 * 8;

typedef unsigned short bf16_t;

#define STAMP(KID, I) do { } while (0)

__device__ __forceinline__ float bf2f(bf16_t u) {
    return __uint_as_float(((unsigned)u) << 16);
}
__device__ __forceinline__ bf16_t f2bf(float f) {
    unsigned u = __float_as_uint(f);
    u += 0x7fffu + ((u >> 16) & 1u);
    return (bf16_t)(u >> 16);
}
__device__ __forceinline__ float rbf(float f) { return bf2f(f2bf(f)); }

__device__ __forceinline__ float2 cadd(float2 a, float2 b) { return make_float2(a.x + b.x, a.y + b.y); }
__device__ __forceinline__ float2 csub(float2 a, float2 b) { return make_float2(a.x - b.x, a.y - b.y); }
__device__ __forceinline__ float2 cmul(float2 a, float2 b) {
    return make_float2(__fmaf_rn(a.x, b.x, -a.y * b.y), __fmaf_rn(a.x, b.y, a.y * b.x));
}
__device__ __forceinline__ float2 cmulc(float2 a, float2 b) {
    return make_float2(__fmaf_rn(a.x, b.x, a.y * b.y), __fmaf_rn(a.y, b.x, -a.x * b.y));
}
template <bool INV>
__device__ __forceinline__ float2 cmul_j(float2 a) {
    return INV ? make_float2(-a.y, a.x) : make_float2(a.y, -a.x);
}

template <bool INV>
__device__ __forceinline__ void dft4(float2 &b0, float2 &b1, float2 &b2, float2 &b3) {
    float2 t0 = cadd(b0, b2), t1 = csub(b0, b2), t2 = cadd(b1, b3), t3 = cmul_j<INV>(csub(b1, b3));
    b0 = cadd(t0, t2); b2 = csub(t0, t2); b1 = cadd(t1, t3); b3 = csub(t1, t3);
}
template <bool INV>
__device__ __forceinline__ void dft5(float2 a[5]) {
    const float c1 = 0.30901699437494745f, c2 = -0.8090169943749473f;
    const float s1 = 0.9510565162951535f,  s2 = 0.5877852522924731f;
    float2 b1 = cadd(a[1], a[4]), b4 = csub(a[1], a[4]);
    float2 b2 = cadd(a[2], a[3]), b3 = csub(a[2], a[3]);
    float2 t1 = make_float2(a[0].x + c1 * b1.x + c2 * b2.x, a[0].y + c1 * b1.y + c2 * b2.y);
    float2 t2 = make_float2(a[0].x + c2 * b1.x + c1 * b2.x, a[0].y + c2 * b1.y + c1 * b2.y);
    float2 u1 = make_float2(s1 * b4.x + s2 * b3.x, s1 * b4.y + s2 * b3.y);
    float2 u2 = make_float2(s2 * b4.x - s1 * b3.x, s2 * b4.y - s1 * b3.y);
    float2 iu1 = cmul_j<INV>(u1), iu2 = cmul_j<INV>(u2);
    a[0] = make_float2(a[0].x + b1.x + b2.x, a[0].y + b1.y + b2.y);
    a[1] = cadd(t1, iu1); a[4] = csub(t1, iu1);
    a[2] = cadd(t2, iu2); a[3] = csub(t2, iu2);
}
template <bool INV>
__device__ __forceinline__ void dft8(float2 a[8]) {
    const float r2 = 0.70710678118654752f;
    float2 e0 = a[0], e1 = a[2], e2 = a[4], e3 = a[6];
    float2 o0 = a[1], o1 = a[3], o2 = a[5], o3 = a[7];
    dft4<INV>(e0, e1, e2, e3);
    dft4<INV>(o0, o1, o2, o3);
    float2 p1, p3;
    if (!INV) {
        p1 = make_float2((o1.x + o1.y) * r2, (o1.y - o1.x) * r2);
        p3 = make_float2((o3.y - o3.x) * r2, -(o3.x + o3.y) * r2);
    } else {
        p1 = make_float2((o1.x - o1.y) * r2, (o1.x + o1.y) * r2);
        p3 = make_float2(-(o3.x + o3.y) * r2, (o3.x - o3.y) * r2);
    }
    float2 p2 = cmul_j<INV>(o2);
    a[0] = cadd(e0, o0); a[4] = csub(e0, o0);
    a[1] = cadd(e1, p1); a[5] = csub(e1, p1);
    a[2] = cadd(e2, p2); a[6] = csub(e2, p2);
    a[3] = cadd(e3, p3); a[7] = csub(e3, p3);
}

template <int R, bool INV> struct Dft;
template <bool INV> struct Dft<4, INV> {
    __device__ __forceinline__ static void run(float2 a[4]) { dft4<INV>(a[0], a[1], a[2], a[3]); }
};
template <bool INV> struct Dft<5, INV> {
    __device__ __forceinline__ static void run(float2 a[5]) { dft5<INV>(a); }
};
template <bool INV> struct Dft<8, INV> {
    __device__ __forceinline__ static void run(float2 a[8]) { dft8<INV>(a); }
};

template <int R, int NT, int NN, int SS, int NROWS, int TWSCALE, bool INV>
__device__ __forceinline__ void stockham_pass(const float2* __restrict__ in, float2* __restrict__ out,
                                              const float2* __restrict__ tw, int tid, int nthreads) {
    constexpr int M = NN / R;
    constexpr int NB = NT / R;
    for (int bt = tid; bt < NROWS * NB; bt += nthreads) {
        const int r   = bt / NB;
        const int rem = bt - r * NB;
        const int p   = rem / SS;
        const int q   = rem - p * SS;
        const float2* rin = in + r * NT;
        float2* rout = out + r * NT;
        float2 a[R];
#pragma unroll
        for (int k = 0; k < R; ++k) a[k] = rin[SS * (p + k * M) + q];
        Dft<R, INV>::run(a);
        if (NN != R) {
#pragma unroll
            for (int k = 1; k < R; ++k) {
                float2 w = tw[p * k * TWSCALE];
                a[k] = INV ? cmulc(a[k], w) : cmul(a[k], w);
            }
        }
#pragma unroll
        for (int k = 0; k < R; ++k) rout[SS * (R * p + k) + q] = a[k];
    }
}

static constexpr int kRowsWG = 8;
static constexpr int kNBlkFFT = kS / kRowsWG;
static constexpr int kNT1 = (kD / 64) * (kF / 64);
static constexpr int kNT2 = (kF / 64) * (kD / 64);
static constexpr int kNTW = (kS / 64) * ((kDF + 63) / 64);
static constexpr int kNTB = (2048 + 640 + 255) / 256;
static constexpr int kNBlkA = kNBlkFFT + kNT1 + kNT2 + kNTW + kNTB;

__device__ __forceinline__ void transpose_tile64(const bf16_t* __restrict__ src, bf16_t* __restrict__ dst,
                                                 int R, int C, int r0, int c0, bf16_t (*tile)[66], int tid) {
#pragma unroll
    for (int i = 0; i < 2; ++i) {
        int u = tid + 256 * i;
        int row = u >> 3, ch = u & 7;
        uint4 v = *reinterpret_cast<const uint4*>(src + (size_t)(r0 + row) * C + c0 + 8 * ch);
        const bf16_t* e = reinterpret_cast<const bf16_t*>(&v);
#pragma unroll
        for (int j = 0; j < 8; ++j) tile[row][8 * ch + j] = e[j];
    }
    __syncthreads();
#pragma unroll
    for (int i = 0; i < 2; ++i) {
        int u = tid + 256 * i;
        int orow = u >> 3, ch = u & 7;
        uint4 v;
        bf16_t* e = reinterpret_cast<bf16_t*>(&v);
#pragma unroll
        for (int j = 0; j < 8; ++j) e[j] = tile[8 * ch + j][orow];
        *reinterpret_cast<uint4*>(dst + (size_t)(c0 + orow) * R + r0 + 8 * ch) = v;
    }
}

__global__ void __launch_bounds__(256) ln_rfft_kernel(const bf16_t* __restrict__ x,
                                                      float2* __restrict__ T,
                                                      const bf16_t* __restrict__ W1, const bf16_t* __restrict__ W2,
                                                      bf16_t* __restrict__ W1t, bf16_t* __restrict__ W2t,
                                                      const bf16_t* __restrict__ w_r, const bf16_t* __restrict__ w_i,
                                                      unsigned* __restrict__ WT,
                                                      float2* __restrict__ tw2048_g, float2* __restrict__ tw640_g) {
    __shared__ float2 tw[640];
    __shared__ __attribute__((aligned(16))) float2 buf[2][kRowsWG * 320];
    const int tid = threadIdx.x;
    const int bid = blockIdx.x;
    if (bid >= kNBlkFFT) {
        int b = bid - kNBlkFFT;
        if (b < kNT1) {
            bf16_t (*tile)[66] = reinterpret_cast<bf16_t (*)[66]>(&buf[0][0]);
            transpose_tile64(W1, W1t, kD, kF, (b / (kF / 64)) * 64, (b % (kF / 64)) * 64, tile, tid);
            return;
        }
        b -= kNT1;
        if (b < kNT2) {
            bf16_t (*tile)[66] = reinterpret_cast<bf16_t (*)[66]>(&buf[0][0]);
            transpose_tile64(W2, W2t, kF, kD, (b / (kD / 64)) * 64, (b % (kD / 64)) * 64, tile, tid);
            return;
        }
        b -= kNT2;
        if (b < kNTW) {
            unsigned (*tile)[65] = reinterpret_cast<unsigned (*)[65]>(&buf[0][0]);
            const int s0 = (b / 6) * 64, l0 = (b % 6) * 64;
            const int ll = tid & 63, sr = tid >> 6;
            if (l0 + ll < kDF) {
#pragma unroll
                for (int i = 0; i < 16; ++i) {
                    const int s = sr + 4 * i;
                    const size_t idx = (size_t)(s0 + s) * kDF + l0 + ll;
                    tile[s][ll] = (unsigned)w_r[idx] | ((unsigned)w_i[idx] << 16);
                }
            }
            __syncthreads();
            const int ss = tid & 63, lr = tid >> 6;
#pragma unroll
            for (int i = 0; i < 16; ++i) {
                const int l = lr + 4 * i;
                if (l0 + l < kDF) WT[(size_t)(l0 + l) * kS + s0 + ss] = tile[ss][l];
            }
            return;
        }
        b -= kNTW;
        {
            const int idx = b * 256 + tid;
            if (idx < 2048) {
                float s, c;
                sincospif(-2.0f * (float)idx / 2048.0f, &s, &c);
                tw2048_g[idx] = make_float2(c, s);
            } else if (idx < 2048 + 640) {
                float s, c;
                sincospif(-2.0f * (float)(idx - 2048) / 640.0f, &s, &c);
                tw640_g[idx - 2048] = make_float2(c, s);
            }
            return;
        }
    }
    const int row0 = bid * kRowsWG;
    STAMP(1, 0);
    const int r = tid >> 5, l32 = tid & 31;
    const bf16_t* xr = x + (size_t)(row0 + r) * kD;
    uint2 xv[5];
#pragma unroll
    for (int i = 0; i < 5; ++i) xv[i] = *reinterpret_cast<const uint2*>(xr + 4 * (l32 + 32 * i));
    for (int i = tid; i < 640; i += 256) {
        float s, c;
        sincospif(-2.0f * (float)i / 640.0f, &s, &c);
        tw[i] = make_float2(c, s);
    }
    float v[20];
    float sum = 0.f;
#pragma unroll
    for (int i = 0; i < 5; ++i) {
        v[4 * i + 0] = bf2f((bf16_t)(xv[i].x & 0xffffu)); v[4 * i + 1] = bf2f((bf16_t)(xv[i].x >> 16));
        v[4 * i + 2] = bf2f((bf16_t)(xv[i].y & 0xffffu)); v[4 * i + 3] = bf2f((bf16_t)(xv[i].y >> 16));
        sum += (v[4 * i] + v[4 * i + 1]) + (v[4 * i + 2] + v[4 * i + 3]);
    }
#pragma unroll
    for (int off = 16; off > 0; off >>= 1) sum += __shfl_xor(sum, off);
    const float mean = sum * (1.0f / kD);
    const float mb = rbf(mean);
    float ss = 0.f;
#pragma unroll
    for (int i = 0; i < 20; ++i) { float d = v[i] - mean; ss += d * d; }
#pragma unroll
    for (int off = 16; off > 0; off >>= 1) ss += __shfl_xor(ss, off);
    const float var = ss * (1.0f / kD);
    const float den = rbf(sqrtf(rbf(rbf(var) + kLnEps)));
    {
        float* bufr = reinterpret_cast<float*>(buf[0]);
#pragma unroll
        for (int i = 0; i < 5; ++i) {
            float4 o;
            o.x = rbf(rbf(v[4 * i + 0] - mb) / den);
            o.y = rbf(rbf(v[4 * i + 1] - mb) / den);
            o.z = rbf(rbf(v[4 * i + 2] - mb) / den);
            o.w = rbf(rbf(v[4 * i + 3] - mb) / den);
            *reinterpret_cast<float4*>(bufr + r * kD + 4 * (l32 + 32 * i)) = o;
        }
    }
    __syncthreads();
    STAMP(1, 1);
    stockham_pass<8, 320, 320, 1, kRowsWG, 2, false>(buf[0], buf[1], tw, tid, 256);
    __syncthreads();
    STAMP(1, 2);
    stockham_pass<8, 320, 40, 8, kRowsWG, 16, false>(buf[1], buf[0], tw, tid, 256);
    __syncthreads();
    STAMP(1, 3);
    stockham_pass<5, 320, 5, 64, kRowsWG, 128, false>(buf[0], buf[1], tw, tid, 256);
    __syncthreads();
    STAMP(1, 4);
    const float2* Z = buf[1];
    for (int k = tid; k < kDF; k += 256) {
        const int km = (320 - k) % 320;
        const int kk = (k == 320) ? 0 : k;
        const float2 w = tw[k];
        float2 outv[kRowsWG];
#pragma unroll
        for (int rr = 0; rr < kRowsWG; ++rr) {
            const float2 Ze = Z[rr * 320 + kk];
            const float2 Zm = Z[rr * 320 + km];
            const float2 E = make_float2(0.5f * (Ze.x + Zm.x), 0.5f * (Ze.y - Zm.y));
            const float dx = Ze.x - Zm.x, dy = Ze.y + Zm.y;
            const float2 O = make_float2(0.5f * dy, -0.5f * dx);
            outv[rr] = cadd(E, cmul(w, O));
        }
        float4* dst = reinterpret_cast<float4*>(T + (size_t)k * kS + row0);
#pragma unroll
        for (int j = 0; j < kRowsWG / 2; ++j)
            dst[j] = make_float4(outv[2 * j].x, outv[2 * j].y, outv[2 * j + 1].x, outv[2 * j + 1].y);
    }
    STAMP(1, 5);
}

__global__ void __launch_bounds__(256) colfft_kernel(const float2* __restrict__ T,
                                                     float2* __restrict__ T2,
                                                     const unsigned* __restrict__ WT,
                                                     const float2* __restrict__ tw2048_g) {
    __shared__ __attribute__((aligned(16))) float2 tw[2048];
    __shared__ __attribute__((aligned(16))) float2 buf[2][2048];
    const int tid = threadIdx.x;
    const int l = blockIdx.x;
    STAMP(2, 0);
    unsigned wp[8];
#pragma unroll
    for (int j = 0; j < 8; ++j) wp[j] = WT[(size_t)l * kS + tid + 256 * j];
    const float4* tw4 = reinterpret_cast<const float4*>(tw2048_g);
    float4* twd = reinterpret_cast<float4*>(tw);
    const float4* col4 = reinterpret_cast<const float4*>(T + (size_t)l * kS);
    float4* b04 = reinterpret_cast<float4*>(buf[0]);
    float4 tv[4], cv[4];
#pragma unroll
    for (int i = 0; i < 4; ++i) { tv[i] = tw4[tid + 256 * i]; cv[i] = col4[tid + 256 * i]; }
#pragma unroll
    for (int i = 0; i < 4; ++i) { twd[tid + 256 * i] = tv[i]; b04[tid + 256 * i] = cv[i]; }
    __syncthreads();
    STAMP(2, 1);
    stockham_pass<8, 2048, 2048, 1, 1, 1, false>(buf[0], buf[1], tw, tid, 256);
    __syncthreads();
    STAMP(2, 2);
    stockham_pass<8, 2048, 256, 8, 1, 8, false>(buf[1], buf[0], tw, tid, 256);
    __syncthreads();
    STAMP(2, 3);
    stockham_pass<8, 2048, 32, 64, 1, 64, false>(buf[0], buf[1], tw, tid, 256);
    __syncthreads();
    STAMP(2, 4);
    {
        const float sc = 1.0f / 2048.0f;
#pragma unroll
        for (int t2 = 0; t2 < 2; ++t2) {
            const int q = tid + 256 * t2;
            float2 a[4];
#pragma unroll
            for (int k = 0; k < 4; ++k) a[k] = buf[1][512 * k + q];
            dft4<false>(a[0], a[1], a[2], a[3]);
#pragma unroll
            for (int k = 0; k < 4; ++k) {
                const int j = 2 * k + t2;
                const float2 wc = make_float2(bf2f((bf16_t)(wp[j] & 0xffffu)) * sc, bf2f((bf16_t)(wp[j] >> 16)) * sc);
                buf[0][512 * k + q] = cmul(a[k], wc);
            }
        }
    }
    __syncthreads();
    STAMP(2, 5);
    stockham_pass<8, 2048, 2048, 1, 1, 1, true>(buf[0], buf[1], tw, tid, 256);
    __syncthreads();
    STAMP(2, 6);
    stockham_pass<8, 2048, 256, 8, 1, 8, true>(buf[1], buf[0], tw, tid, 256);
    __syncthreads();
    STAMP(2, 7);
    stockham_pass<8, 2048, 32, 64, 1, 64, true>(buf[0], buf[1], tw, tid, 256);
    __syncthreads();
    STAMP(2, 8);
#pragma unroll
    for (int t2 = 0; t2 < 2; ++t2) {
        const int q = tid + 256 * t2;
        float2 a[4];
#pragma unroll
        for (int k = 0; k < 4; ++k) a[k] = buf[1][512 * k + q];
        dft4<true>(a[0], a[1], a[2], a[3]);
#pragma unroll
        for (int k = 0; k < 4; ++k) T2[(size_t)l * kS + 512 * k + q] = a[k];
    }
    STAMP(2, 9);
}

__global__ void __launch_bounds__(256) irfft_ln_kernel(const bf16_t* __restrict__ x,
                                                       const float2* __restrict__ T2,
                                                       const float2* __restrict__ tw640_g,
                                                       bf16_t* __restrict__ hout,
                                                       bf16_t* __restrict__ yln2) {
    __shared__ float2 tw[640];
    __shared__ __attribute__((aligned(16))) float2 X[kRowsWG][322];
    __shared__ __attribute__((aligned(16))) float2 buf[2][kRowsWG * 320];
    const int tid = threadIdx.x;
    const int row0 = blockIdx.x * kRowsWG;
    STAMP(3, 0);
    const int r = tid >> 5, l32 = tid & 31;
    const bf16_t* xrow = x + (size_t)(row0 + r) * kD;
    uint2 xv[5];
#pragma unroll
    for (int i = 0; i < 5; ++i) xv[i] = *reinterpret_cast<const uint2*>(xrow + 4 * (l32 + 32 * i));
    for (int i = tid; i < 640; i += 256) tw[i] = tw640_g[i];
    for (int k = tid; k < kDF; k += 256) {
        const float4* src = reinterpret_cast<const float4*>(T2 + (size_t)k * kS + row0);
        float4 v0 = src[0], v1 = src[1], v2 = src[2], v3 = src[3];
        X[0][k] = make_float2(v0.x, v0.y); X[1][k] = make_float2(v0.z, v0.w);
        X[2][k] = make_float2(v1.x, v1.y); X[3][k] = make_float2(v1.z, v1.w);
        X[4][k] = make_float2(v2.x, v2.y); X[5][k] = make_float2(v2.z, v2.w);
        X[6][k] = make_float2(v3.x, v3.y); X[7][k] = make_float2(v3.z, v3.w);
    }
    __syncthreads();
    STAMP(3, 1);
    for (int task = tid; task < kRowsWG * 320; task += 256) {
        const int rr = task / 320, k = task - rr * 320;
        float2 Xk = X[rr][k];
        float2 Xm = X[rr][320 - k];
        if (k == 0) { Xk.y = 0.f; Xm.y = 0.f; }
        const float2 E = make_float2(0.5f * (Xk.x + Xm.x), 0.5f * (Xk.y - Xm.y));
        const float2 d = make_float2(Xk.x - Xm.x, Xk.y + Xm.y);
        float2 O = cmulc(d, tw[k]);
        O.x *= 0.5f; O.y *= 0.5f;
        buf[0][rr * 320 + k] = make_float2(E.x - O.y, E.y + O.x);
    }
    __syncthreads();
    STAMP(3, 2);
    stockham_pass<8, 320, 320, 1, kRowsWG, 2, true>(buf[0], buf[1], tw, tid, 256);
    __syncthreads();
    STAMP(3, 3);
    stockham_pass<8, 320, 40, 8, kRowsWG, 16, true>(buf[1], buf[0], tw, tid, 256);
    __syncthreads();
    STAMP(3, 4);
    stockham_pass<5, 320, 5, 64, kRowsWG, 128, true>(buf[0], buf[1], tw, tid, 256);
    __syncthreads();
    STAMP(3, 5);
    const float* zr = reinterpret_cast<const float*>(buf[1] + r * 320);
    bf16_t* hrow = hout + (size_t)(row0 + r) * kD;
    bf16_t* yrow = yln2 + (size_t)(row0 + r) * kD;
    float hv[20];
    float sum = 0.f;
#pragma unroll
    for (int i = 0; i < 5; ++i) {
        const int c = l32 + 32 * i;
        const float4 zv = *reinterpret_cast<const float4*>(zr + 4 * c);
        const float xa = bf2f((bf16_t)(xv[i].x & 0xffffu)), xb = bf2f((bf16_t)(xv[i].x >> 16));
        const float xc = bf2f((bf16_t)(xv[i].y & 0xffffu)), xd = bf2f((bf16_t)(xv[i].y >> 16));
        const float h0 = rbf(xa + rbf(zv.x * (1.0f / 320.0f)));
        const float h1 = rbf(xb + rbf(zv.y * (1.0f / 320.0f)));
        const float h2 = rbf(xc + rbf(zv.z * (1.0f / 320.0f)));
        const float h3 = rbf(xd + rbf(zv.w * (1.0f / 320.0f)));
        hv[4 * i] = h0; hv[4 * i + 1] = h1; hv[4 * i + 2] = h2; hv[4 * i + 3] = h3;
        uint2 hp;
        hp.x = (unsigned)f2bf(h0) | ((unsigned)f2bf(h1) << 16);
        hp.y = (unsigned)f2bf(h2) | ((unsigned)f2bf(h3) << 16);
        *reinterpret_cast<uint2*>(hrow + 4 * c) = hp;
        sum += (h0 + h1) + (h2 + h3);
    }
#pragma unroll
    for (int off = 16; off > 0; off >>= 1) sum += __shfl_xor(sum, off);
    const float mean = sum * (1.0f / kD);
    const float mb = rbf(mean);
    float ss = 0.f;
#pragma unroll
    for (int i = 0; i < 20; ++i) { float d = hv[i] - mean; ss += d * d; }
#pragma unroll
    for (int off = 16; off > 0; off >>= 1) ss += __shfl_xor(ss, off);
    const float var = ss * (1.0f / kD);
    const float den = rbf(sqrtf(rbf(rbf(var) + kLnEps)));
    float y1[20];
    float sum2 = 0.f;
#pragma unroll
    for (int i = 0; i < 20; ++i) { y1[i] = rbf(rbf(hv[i] - mb) / den); sum2 += y1[i]; }
#pragma unroll
    for (int off = 16; off > 0; off >>= 1) sum2 += __shfl_xor(sum2, off);
    const float mean2 = sum2 * (1.0f / kD);
    const float mb2 = rbf(mean2);
    float ss2 = 0.f;
#pragma unroll
    for (int i = 0; i < 20; ++i) { float d = y1[i] - mean2; ss2 += d * d; }
#pragma unroll
    for (int off = 16; off > 0; off >>= 1) ss2 += __shfl_xor(ss2, off);
    const float var2 = ss2 * (1.0f / kD);
    const float den2 = rbf(sqrtf(rbf(rbf(var2) + kLnEps)));
#pragma unroll
    for (int i = 0; i < 5; ++i) {
        const int c = l32 + 32 * i;
        uint2 yp;
        yp.x = (unsigned)f2bf(rbf(y1[4 * i] - mb2) / den2) | ((unsigned)f2bf(rbf(y1[4 * i + 1] - mb2) / den2) << 16);
        yp.y = (unsigned)f2bf(rbf(y1[4 * i + 2] - mb2) / den2) | ((unsigned)f2bf(rbf(y1[4 * i + 3] - mb2) / den2) << 16);
        *reinterpret_cast<uint2*>(yrow + 4 * c) = yp;
    }
    STAMP(3, 6);
}

using bf16x8 = __attribute__((ext_vector_type(8))) __bf16;
using f32x16 = __attribute__((ext_vector_type(16))) float;
typedef unsigned __attribute__((ext_vector_type(4))) u32x4;
typedef unsigned __attribute__((ext_vector_type(2))) u32x2;

#define WAIT_V(n) asm volatile("s_waitcnt vmcnt(%0)" ::"n"(n) : "memory")
#define WAIT_L(n) asm volatile("s_waitcnt lgkmcnt(%0)" ::"n"(n) : "memory")
#define RAW_BARRIER() do { WAIT_L(0); __builtin_amdgcn_s_barrier(); } while (0)

__device__ __forceinline__ float gelu_tanh_f(float t) {
    const float x2 = t * __fmaf_rn(0.0713548162726008f, t * t, 1.5957691216057308f);
    return t * __frcp_rn(1.0f + __expf(-x2));
}

template <int TM, int TN, int WM, int WN, int NBUF, int EPI>
__global__ void __launch_bounds__(256) gemm_kernel(const bf16_t* __restrict__ A, int lda,
                                                   const bf16_t* __restrict__ Bt, int ldb, int K,
                                                   const bf16_t* __restrict__ bias,
                                                   const bf16_t* __restrict__ resid,
                                                   bf16_t* __restrict__ C, int ldc) {
    constexpr int BK = 64;
    constexpr int D = NBUF - 1;
    constexpr int WTM = TM / WM, WTN = TN / WN;
    constexpr int MB = WTM / 32, NB = WTN / 32;
    constexpr int A_TILE_B = TM * 128, B_TILE_B = TN * 128, STAGE_B = A_TILE_B + B_TILE_B;
    constexpr int GA = TM / 32, GB = TN / 32;
    constexpr int GL = GA + GB;
    static_assert(WM * WN == 4, "4 waves");
    static_assert((D - 1) * GL <= 63, "vmcnt range");
    static_assert(4 * WTM * (WTN * 2 + 16) <= NBUF * STAGE_B, "epilogue staging fits");
    __shared__ __attribute__((aligned(1024))) char shm[NBUF * STAGE_B];

    const int tid = threadIdx.x, lane = tid & 63, wave = tid >> 6;
    const int wm = wave / WN, wn = wave % WN;
    const int m0 = blockIdx.y * TM, n0 = blockIdx.x * TN;
    const int r32 = lane & 31, hh = lane >> 5;

    int aoff[GA], boff[GB];
#pragma unroll
    for (int i = 0; i < GA; ++i) {
        const int row = 8 * (wave * GA + i) + (lane >> 3);
        const int c = (lane & 7) ^ ((row >> 1) & 7);
        aoff[i] = (m0 + row) * lda + 8 * c;
    }
#pragma unroll
    for (int i = 0; i < GB; ++i) {
        const int row = 8 * (wave * GB + i) + (lane >> 3);
        const int c = (lane & 7) ^ ((row >> 1) & 7);
        boff[i] = (n0 + row) * ldb + 8 * c;
    }
#define GLDS_STAGE(BUF, KT)                                                                        \
    do {                                                                                           \
        char* sa_ = shm + (BUF) * STAGE_B;                                                         \
        _Pragma("unroll") for (int i = 0; i < GA; ++i)                                             \
            __builtin_amdgcn_global_load_lds((const unsigned*)(A + aoff[i] + (KT) * BK),           \
                                             (unsigned*)(sa_ + (wave * GA + i) * 1024), 16, 0, 0); \
        _Pragma("unroll") for (int i = 0; i < GB; ++i)                                             \
            __builtin_amdgcn_global_load_lds((const unsigned*)(Bt + boff[i] + (KT) * BK),          \
                                             (unsigned*)(sa_ + A_TILE_B + (wave * GB + i) * 1024), 16, 0, 0); \
    } while (0)

    f32x16 acc[MB][NB];
#pragma unroll
    for (int i = 0; i < MB; ++i)
#pragma unroll
        for (int j = 0; j < NB; ++j)
#pragma unroll
            for (int e = 0; e < 16; ++e) acc[i][j][e] = 0.f;

    const int sw = (r32 >> 1) & 7;
    const int KT = K / BK;
    STAMP(4 + EPI, 0);
#pragma unroll
    for (int i = 0; i < D; ++i) GLDS_STAGE(i, i);
    WAIT_V((D - 1) * GL);
    RAW_BARRIER();
    STAMP(4 + EPI, 1);
    for (int kt = 0; kt < KT; ++kt) {
        const int cur = kt % NBUF;
        if (kt + D < KT) GLDS_STAGE((kt + D) % NBUF, kt + D);
        const char* sa = shm + cur * STAGE_B;
        const char* sb = sa + A_TILE_B;
#pragma unroll
        for (int kk = 0; kk < 4; ++kk) {
            const int coff = 16 * ((2 * kk + hh) ^ sw);
            bf16x8 af[MB], bfv[NB];
#pragma unroll
            for (int mi = 0; mi < MB; ++mi)
                af[mi] = *reinterpret_cast<const bf16x8*>(sa + (wm * WTM + mi * 32 + r32) * 128 + coff);
#pragma unroll
            for (int ni = 0; ni < NB; ++ni)
                bfv[ni] = *reinterpret_cast<const bf16x8*>(sb + (wn * WTN + ni * 32 + r32) * 128 + coff);
#pragma unroll
            for (int mi = 0; mi < MB; ++mi)
#pragma unroll
                for (int ni = 0; ni < NB; ++ni)
                    acc[mi][ni] = __builtin_amdgcn_mfma_f32_32x32x16_bf16(bfv[ni], af[mi], acc[mi][ni], 0, 0, 0);
        }
        if (kt + D < KT) WAIT_V((D - 1) * GL); else WAIT_V(0);
        RAW_BARRIER();
    }
    STAMP(4 + EPI, 2);
#undef GLDS_STAGE
    constexpr int SSTR = WTN * 2 + 16;
    char* stg = shm + wave * (WTM * SSTR);
#pragma unroll
    for (int ni = 0; ni < NB; ++ni) {
#pragma unroll
        for (int g = 0; g < 4; ++g) {
            const int nl = ni * 32 + 8 * g + 4 * hh;
            const u32x2 bb = *reinterpret_cast<const u32x2*>(bias + n0 + wn * WTN + nl);
            const float b0 = bf2f((bf16_t)(bb[0] & 0xffffu)), b1 = bf2f((bf16_t)(bb[0] >> 16));
            const float b2 = bf2f((bf16_t)(bb[1] & 0xffffu)), b3 = bf2f((bf16_t)(bb[1] >> 16));
#pragma unroll
            for (int mi = 0; mi < MB; ++mi) {
                float t0 = rbf(rbf(acc[mi][ni][4 * g + 0]) + b0);
                float t1 = rbf(rbf(acc[mi][ni][4 * g + 1]) + b1);
                float t2 = rbf(rbf(acc[mi][ni][4 * g + 2]) + b2);
                float t3 = rbf(rbf(acc[mi][ni][4 * g + 3]) + b3);
                if (EPI == 0) { t0 = gelu_tanh_f(t0); t1 = gelu_tanh_f(t1); t2 = gelu_tanh_f(t2); t3 = gelu_tanh_f(t3); }
                u32x2 pk;
                pk[0] = (unsigned)f2bf(t0) | ((unsigned)f2bf(t1) << 16);
                pk[1] = (unsigned)f2bf(t2) | ((unsigned)f2bf(t3) << 16);
                *reinterpret_cast<u32x2*>(stg + (mi * 32 + r32) * SSTR + nl * 2) = pk;
            }
        }
    }
    WAIT_L(0);
    constexpr int CPR = WTN / 8;
    constexpr int NU = WTM * CPR / 64;
#pragma unroll
    for (int i = 0; i < NU; ++i) {
        const int u = lane + 64 * i;
        const int row = u / CPR, ch = u % CPR;
        const u32x4 v = *reinterpret_cast<const u32x4*>(stg + row * SSTR + 16 * ch);
        const size_t go = (size_t)(m0 + wm * WTM + row) * ldc + n0 + wn * WTN + 8 * ch;
        if (EPI == 0) {
            *reinterpret_cast<u32x4*>(C + go) = v;
        } else {
            const u32x4 hr = *reinterpret_cast<const u32x4*>(resid + go);
            u32x4 o;
#pragma unroll
            for (int q = 0; q < 4; ++q) {
                const float lo = bf2f((bf16_t)(hr[q] & 0xffffu)) + bf2f((bf16_t)(v[q] & 0xffffu));
                const float hi = bf2f((bf16_t)(hr[q] >> 16)) + bf2f((bf16_t)(v[q] >> 16));
                o[q] = (unsigned)f2bf(lo) | ((unsigned)f2bf(hi) << 16);
            }
            *reinterpret_cast<u32x4*>(C + go) = o;
        }
    }
    STAMP(4 + EPI, 3);
}

template <int TM, int TN, int WM, int WN, int EPI>
__global__ void __launch_bounds__(256) gemm_bt_kernel(const bf16_t* __restrict__ A, int lda,
                                                      const bf16_t* __restrict__ Bt, int ldb, int K,
                                                      const bf16_t* __restrict__ bias,
                                                      const bf16_t* __restrict__ resid,
                                                      bf16_t* __restrict__ C, int ldc) {
    constexpr int BK  = 64;
    constexpr int LDS_STRIDE = BK + 8;
    constexpr int WTM = TM / WM, WTN = TN / WN;
    constexpr int MB  = WTM / 32, NB = WTN / 32;
    constexpr int AU  = TM * BK / 8 / 256;
    constexpr int BU  = TN * BK / 8 / 256;
    __shared__ __attribute__((aligned(16))) bf16_t As[2][TM][LDS_STRIDE];
    __shared__ __attribute__((aligned(16))) bf16_t Bs[2][TN][LDS_STRIDE];
    const int tid = threadIdx.x, lane = tid & 63, wave = tid >> 6;
    const int wm = wave / WN, wn = wave % WN;
    const int m0 = blockIdx.y * TM, n0 = blockIdx.x * TN;
    u32x4 ra0, ra1, ra2, ra3, ra4, ra5, ra6, ra7, rb0, rb1;
    ra1 = ra2 = ra3 = ra4 = ra5 = ra6 = ra7 = rb1 = (u32x4){0u, 0u, 0u, 0u};
#define A_ADDR(I, K0) (A + (size_t)(m0 + ((tid + 256 * (I)) >> 3)) * lda + (K0) + 8 * ((tid + 256 * (I)) & 7))
#define B_ADDR(I, K0) (Bt + (size_t)(n0 + ((tid + 256 * (I)) >> 3)) * ldb + (K0) + 8 * ((tid + 256 * (I)) & 7))
#define AS_PTR(BUF, I) (&As[(BUF)][(tid + 256 * (I)) >> 3][8 * ((tid + 256 * (I)) & 7)])
#define BS_PTR(BUF, I) (&Bs[(BUF)][(tid + 256 * (I)) >> 3][8 * ((tid + 256 * (I)) & 7)])
#define GLOAD(K0)                                                       \
    do {                                                                \
        ra0 = *reinterpret_cast<const u32x4*>(A_ADDR(0, K0));           \
        if (AU > 1) ra1 = *reinterpret_cast<const u32x4*>(A_ADDR(1, K0)); \
        if (AU > 2) ra2 = *reinterpret_cast<const u32x4*>(A_ADDR(2, K0)); \
        if (AU > 3) ra3 = *reinterpret_cast<const u32x4*>(A_ADDR(3, K0)); \
        if (AU > 4) ra4 = *reinterpret_cast<const u32x4*>(A_ADDR(4, K0)); \
        if (AU > 5) ra5 = *reinterpret_cast<const u32x4*>(A_ADDR(5, K0)); \
        if (AU > 6) ra6 = *reinterpret_cast<const u32x4*>(A_ADDR(6, K0)); \
        if (AU > 7) ra7 = *reinterpret_cast<const u32x4*>(A_ADDR(7, K0)); \
        rb0 = *reinterpret_cast<const u32x4*>(B_ADDR(0, K0));           \
        if (BU > 1) rb1 = *reinterpret_cast<const u32x4*>(B_ADDR(1, K0)); \
    } while (0)
#define SSTORE(BUF)                                                     \
    do {                                                                \
        *reinterpret_cast<u32x4*>(AS_PTR(BUF, 0)) = ra0;                \
        if (AU > 1) *reinterpret_cast<u32x4*>(AS_PTR(BUF, 1)) = ra1;    \
        if (AU > 2) *reinterpret_cast<u32x4*>(AS_PTR(BUF, 2)) = ra2;    \
        if (AU > 3) *reinterpret_cast<u32x4*>(AS_PTR(BUF, 3)) = ra3;    \
        if (AU > 4) *reinterpret_cast<u32x4*>(AS_PTR(BUF, 4)) = ra4;    \
        if (AU > 5) *reinterpret_cast<u32x4*>(AS_PTR(BUF, 5)) = ra5;    \
        if (AU > 6) *reinterpret_cast<u32x4*>(AS_PTR(BUF, 6)) = ra6;    \
        if (AU > 7) *reinterpret_cast<u32x4*>(AS_PTR(BUF, 7)) = ra7;    \
        *reinterpret_cast<u32x4*>(BS_PTR(BUF, 0)) = rb0;                \
        if (BU > 1) *reinterpret_cast<u32x4*>(BS_PTR(BUF, 1)) = rb1;    \
    } while (0)
    f32x16 acc[MB][NB];
#pragma unroll
    for (int i = 0; i < MB; ++i)
#pragma unroll
        for (int j = 0; j < NB; ++j)
#pragma unroll
            for (int e = 0; e < 16; ++e) acc[i][j][e] = 0.f;
    const int KT = K / BK;
    GLOAD(0);
    SSTORE(0);
    __syncthreads();
    for (int kt = 0; kt < KT; ++kt) {
        const int cur = kt & 1;
        if (kt + 1 < KT) GLOAD((kt + 1) * BK);
#pragma unroll
        for (int kk = 0; kk < 4; ++kk) {
            bf16x8 af[MB], bfv[NB];
#pragma unroll
            for (int mi = 0; mi < MB; ++mi)
                af[mi] = *reinterpret_cast<const bf16x8*>(
                    &As[cur][wm * WTM + mi * 32 + (lane & 31)][kk * 16 + (lane >> 5) * 8]);
#pragma unroll
            for (int ni = 0; ni < NB; ++ni)
                bfv[ni] = *reinterpret_cast<const bf16x8*>(
                    &Bs[cur][wn * WTN + ni * 32 + (lane & 31)][kk * 16 + (lane >> 5) * 8]);
#pragma unroll
            for (int mi = 0; mi < MB; ++mi)
#pragma unroll
                for (int ni = 0; ni < NB; ++ni)
                    acc[mi][ni] = __builtin_amdgcn_mfma_f32_32x32x16_bf16(af[mi], bfv[ni], acc[mi][ni], 0, 0, 0);
        }
        if (kt + 1 < KT) SSTORE(cur ^ 1);
        __syncthreads();
    }
#undef GLOAD
#undef SSTORE
#undef A_ADDR
#undef B_ADDR
#undef AS_PTR
#undef BS_PTR
#pragma unroll
    for (int mi = 0; mi < MB; ++mi)
#pragma unroll
        for (int ni = 0; ni < NB; ++ni) {
            const int col = n0 + wn * WTN + ni * 32 + (lane & 31);
            const float bcol = bf2f(bias[col]);
#pragma unroll
            for (int e = 0; e < 16; ++e) {
                const int row = m0 + wm * WTM + mi * 32 + (e & 3) + 8 * (e >> 2) + 4 * (lane >> 5);
                const float accf = rbf(acc[mi][ni][e]);
                const float t = rbf(accf + bcol);
                if (EPI == 0) {
                    C[(size_t)row * ldc + col] = f2bf(gelu_tanh_f(t));
                } else {
                    const float hv = bf2f(resid[(size_t)row * ldc + col]);
                    C[(size_t)row * ldc + col] = f2bf(hv + t);
                }
            }
        }
}

extern "C" void kernel_launch(void* const* d_in, const int* in_sizes, int n_in,
                              void* d_out, int out_size, void* d_ws, size_t ws_size,
                              hipStream_t stream) {
    (void)in_sizes; (void)n_in; (void)out_size; (void)ws_size;
    const bf16_t* A_x  = (const bf16_t*)d_in[0];
    const bf16_t* A_wr = (const bf16_t*)d_in[1];
    const bf16_t* A_wi = (const bf16_t*)d_in[2];
    const bf16_t* B_w1 = (const bf16_t*)d_in[4];
    const bf16_t* B_b1 = (const bf16_t*)d_in[5];
    const bf16_t* B_w2 = (const bf16_t*)d_in[6];
    const bf16_t* B_b2 = (const bf16_t*)d_in[7];
    bf16_t* out = (bf16_t*)d_out;

    char* ws = (char*)d_ws;
    float2* T      = (float2*)(ws + OFF_T);
    float2* T2     = (float2*)(ws + OFF_T2);
    bf16_t* hbuf   = (bf16_t*)(ws + OFF_H);
    bf16_t* yln2   = (bf16_t*)(ws + OFF_YLN2);
    bf16_t* Hbuf   = (bf16_t*)(ws + OFF_G);
    bf16_t* W1t    = (bf16_t*)(ws + OFF_W1T);
    bf16_t* W2t    = (bf16_t*)(ws + OFF_W2T);
    float2* tw2048 = (float2*)(ws + OFF_TW2048);
    float2* tw640  = (float2*)(ws + OFF_TW640);
    unsigned* WT   = (unsigned*)(ws + OFF_WT);

    ln_rfft_kernel<<<kNBlkA, 256, 0, stream>>>(A_x, T, B_w1, B_w2, W1t, W2t, A_wr, A_wi, WT, tw2048, tw640);
    colfft_kernel<<<kDF, 256, 0, stream>>>(T, T2, WT, tw2048);
    irfft_ln_kernel<<<kS / kRowsWG, 256, 0, stream>>>(A_x, T2, tw640, hbuf, yln2);
    gemm_bt_kernel<256, 64, 4, 1, 0><<<dim3(kF / 64, kS / 256), 256, 0, stream>>>(
        yln2, kD, W1t, kD, kD, B_b1, nullptr, Hbuf, kF);
    gemm_bt_kernel<128, 64, 2, 2, 1><<<dim3(kD / 64, kS / 128), 256, 0, stream>>>(
        Hbuf, kF, W2t, kF, kF, B_b2, hbuf, out, kD);
}
```

```cpp
#include <hip/hip_runtime.h>
#include <hip/hip_bf16.h>
#include <stdint.h>

static constexpr int kS  = 2048;
static constexpr int kD  = 640;
static constexpr int kDF = 321;
static constexpr int kF  = 1728;
static constexpr float kLnEps = 1e-5f;

static constexpr size_t OFF_T     = 0;
static constexpr size_t OFF_T2    = OFF_T   + (size_t)kDF * kS * 8;
static constexpr size_t OFF_H     = OFF_T2  + (size_t)kDF * kS * 8;
static constexpr size_t OFF_YLN2  = OFF_H   + (size_t)kS * kD * 2;
static constexpr size_t OFF_G     = OFF_YLN2+ (size_t)kS * kD * 2;
static constexpr size_t OFF_W1T   = OFF_G   + (size_t)kS * kF * 2;
static constexpr size_t OFF_W2T   = OFF_W1T + (size_t)kF * kD * 2;
static constexpr size_t OFF_TW2048= OFF_W2T + (size_t)kD * kF * 2;
static constexpr size_t OFF_TW640 = OFF_TW2048 + (size_t)2048 * 8;
static constexpr size_t OFF_WT    = OFF_TW640 + (size_t)640 * 8;

typedef unsigned short bf16_t;

#define STAMP(KID, I) do { } while (0)

__device__ __forceinline__ float bf2f(bf16_t u) {
    return __uint_as_float(((unsigned)u) << 16);
}
__device__ __forceinline__ bf16_t f2bf(float f) {
    unsigned u = __float_as_uint(f);
    u += 0x7fffu + ((u >> 16) & 1u);
    return (bf16_t)(u >> 16);
}
__device__ __forceinline__ float rbf(float f) { return bf2f(f2bf(f)); }

__device__ __forceinline__ float2 cadd(float2 a, float2 b) { return make_float2(a.x + b.x, a.y + b.y); }
__device__ __forceinline__ float2 csub(float2 a, float2 b) { return make_float2(a.x - b.x, a.y - b.y); }
__device__ __forceinline__ float2 cmul(float2 a, float2 b) {
    return make_float2(__fmaf_rn(a.x, b.x, -a.y * b.y), __fmaf_rn(a.x, b.y, a.y * b.x));
}
__device__ __forceinline__ float2 cmulc(float2 a, float2 b) {
    return make_float2(__fmaf_rn(a.x, b.x, a.y * b.y), __fmaf_rn(a.y, b.x, -a.x * b.y));
}
template <bool INV>
__device__ __forceinline__ float2 cmul_j(float2 a) {
    return INV ? make_float2(-a.y, a.x) : make_float2(a.y, -a.x);
}

template <bool INV>
__device__ __forceinline__ void dft4(float2 &b0, float2 &b1, float2 &b2, float2 &b3) {
    float2 t0 = cadd(b0, b2), t1 = csub(b0, b2), t2 = cadd(b1, b3), t3 = cmul_j<INV>(csub(b1, b3));
    b0 = cadd(t0, t2); b2 = csub(t0, t2); b1 = cadd(t1, t3); b3 = csub(t1, t3);
}
template <bool INV>
__device__ __forceinline__ void dft5(float2 a[5]) {
    const float c1 = 0.30901699437494745f, c2 = -0.8090169943749473f;
    const float s1 = 0.9510565162951535f,  s2 = 0.5877852522924731f;
    float2 b1 = cadd(a[1], a[4]), b4 = csub(a[1], a[4]);
    float2 b2 = cadd(a[2], a[3]), b3 = csub(a[2], a[3]);
    float2 t1 = make_float2(a[0].x + c1 * b1.x + c2 * b2.x, a[0].y + c1 * b1.y + c2 * b2.y);
    float2 t2 = make_float2(a[0].x + c2 * b1.x + c1 * b2.x, a[0].y + c2 * b1.y + c1 * b2.y);
    float2 u1 = make_float2(s1 * b4.x + s2 * b3.x, s1 * b4.y + s2 * b3.y);
    float2 u2 = make_float2(s2 * b4.x - s1 * b3.x, s2 * b4.y - s1 * b3.y);
    float2 iu1 = cmul_j<INV>(u1), iu2 = cmul_j<INV>(u2);
    a[0] = make_float2(a[0].x + b1.x + b2.x, a[0].y + b1.y + b2.y);
    a[1] = cadd(t1, iu1); a[4] = csub(t1, iu1);
    a[2] = cadd(t2, iu2); a[3] = csub(t2, iu2);
}
template <bool INV>
__device__ __forceinline__ void dft8(float2 a[8]) {
    const float r2 = 0.70710678118654752f;
    float2 e0 = a[0], e1 = a[2], e2 = a[4], e3 = a[6];
    float2 o0 = a[1], o1 = a[3], o2 = a[5], o3 = a[7];
    dft4<INV>(e0, e1, e2, e3);
    dft4<INV>(o0, o1, o2, o3);
    float2 p1, p3;
    if (!INV) {
        p1 = make_float2((o1.x + o1.y) * r2, (o1.y - o1.x) * r2);
        p3 = make_float2((o3.y - o3.x) * r2, -(o3.x + o3.y) * r2);
    } else {
        p1 = make_float2((o1.x - o1.y) * r2, (o1.x + o1.y) * r2);
        p3 = make_float2(-(o3.x + o3.y) * r2, (o3.x - o3.y) * r2);
    }
    float2 p2 = cmul_j<INV>(o2);
    a[0] = cadd(e0, o0); a[4] = csub(e0, o0);
    a[1] = cadd(e1, p1); a[5] = csub(e1, p1);
    a[2] = cadd(e2, p2); a[6] = csub(e2, p2);
    a[3] = cadd(e3, p3); a[7] = csub(e3, p3);
}

template <int R, bool INV> struct Dft;
template <bool INV> struct Dft<4, INV> {
    __device__ __forceinline__ static void run(float2 a[4]) { dft4<INV>(a[0], a[1], a[2], a[3]); }
};
template <bool INV> struct Dft<5, INV> {
    __device__ __forceinline__ static void run(float2 a[5]) { dft5<INV>(a); }
};
template <bool INV> struct Dft<8, INV> {
    __device__ __forceinline__ static void run(float2 a[8]) { dft8<INV>(a); }
};

template <int R, int NT, int NN, int SS, int NROWS, int TWSCALE, bool INV>
__device__ __forceinline__ void stockham_pass(const float2* __restrict__ in, float2* __restrict__ out,
                                              const float2* __restrict__ tw, int tid, int nthreads) {
    constexpr int M = NN / R;
    constexpr int NB = NT / R;
    for (int bt = tid; bt < NROWS * NB; bt += nthreads) {
        const int r   = bt / NB;
        const int rem = bt - r * NB;
        const int p   = rem / SS;
        const int q   = rem - p * SS;
        const float2* rin = in + r * NT;
        float2* rout = out + r * NT;
        float2 a[R];
#pragma unroll
        for (int k = 0; k < R; ++k) a[k] = rin[SS * (p + k * M) + q];
        Dft<R, INV>::run(a);
        if (NN != R) {
#pragma unroll
            for (int k = 1; k < R; ++k) {
                float2 w = tw[p * k * TWSCALE];
                a[k] = INV ? cmulc(a[k], w) : cmul(a[k], w);
            }
        }
#pragma unroll
        for (int k = 0; k < R; ++k) rout[SS * (R * p + k) + q] = a[k];
    }
}

static constexpr int kRowsWG = 8;
static constexpr int kNBlkFFT = kS / kRowsWG;
static constexpr int kNT1 = (kD / 64) * (kF / 64);
static constexpr int kNT2 = (kF / 64) * (kD / 64);
static constexpr int kNTW = (kS / 64) * ((kDF + 63) / 64);
static constexpr int kNTB = (2048 + 640 + 255) / 256;
static constexpr int kNBlkA = kNBlkFFT + kNT1 + kNT2 + kNTW + kNTB;

__device__ __forceinline__ void transpose_tile64(const bf16_t* __restrict__ src, bf16_t* __restrict__ dst,
                                                 int R, int C, int r0, int c0, bf16_t (*tile)[66], int tid) {
#pragma unroll
    for (int i = 0; i < 2; ++i) {
        int u = tid + 256 * i;
        int row = u >> 3, ch = u & 7;
        uint4 v = *reinterpret_cast<const uint4*>(src + (size_t)(r0 + row) * C + c0 + 8 * ch);
        const bf16_t* e = reinterpret_cast<const bf16_t*>(&v);
#pragma unroll
        for (int j = 0; j < 8; ++j) tile[row][8 * ch + j] = e[j];
    }
    __syncthreads();
#pragma unroll
    for (int i = 0; i < 2; ++i) {
        int u = tid + 256 * i;
        int orow = u >> 3, ch = u & 7;
        uint4 v;
        bf16_t* e = reinterpret_cast<bf16_t*>(&v);
#pragma unroll
        for (int j = 0; j < 8; ++j) e[j] = tile[8 * ch + j][orow];
        *reinterpret_cast<uint4*>(dst + (size_t)(c0 + orow) * R + r0 + 8 * ch) = v;
    }
}

__global__ void __launch_bounds__(256) ln_rfft_kernel(const bf16_t* __restrict__ x,
                                                      float2* __restrict__ T,
                                                      const bf16_t* __restrict__ W1, const bf16_t* __restrict__ W2,
                                                      bf16_t* __restrict__ W1t, bf16_t* __restrict__ W2t,
                                                      const bf16_t* __restrict__ w_r, const bf16_t* __restrict__ w_i,
                                                      unsigned* __restrict__ WT,
                                                      float2* __restrict__ tw2048_g, float2* __restrict__ tw640_g) {
    __shared__ float2 tw[640];
    __shared__ __attribute__((aligned(16))) float2 buf[2][kRowsWG * 320];
    const int tid = threadIdx.x;
    const int bid = blockIdx.x;
    if (bid >= kNBlkFFT) {
        int b = bid - kNBlkFFT;
        if (b < kNT1) {
            bf16_t (*tile)[66] = reinterpret_cast<bf16_t (*)[66]>(&buf[0][0]);
            transpose_tile64(W1, W1t, kD, kF, (b / (kF / 64)) * 64, (b % (kF / 64)) * 64, tile, tid);
            return;
        }
        b -= kNT1;
        if (b < kNT2) {
            bf16_t (*tile)[66] = reinterpret_cast<bf16_t (*)[66]>(&buf[0][0]);
            transpose_tile64(W2, W2t, kF, kD, (b / (kD / 64)) * 64, (b % (kD / 64)) * 64, tile, tid);
            return;
        }
        b -= kNT2;
        if (b < kNTW) {
            unsigned (*tile)[65] = reinterpret_cast<unsigned (*)[65]>(&buf[0][0]);
            const int s0 = (b / 6) * 64, l0 = (b % 6) * 64;
            const int ll = tid & 63, sr = tid >> 6;
            if (l0 + ll < kDF) {
#pragma unroll
                for (int i = 0; i < 16; ++i) {
                    const int s = sr + 4 * i;
                    const size_t idx = (size_t)(s0 + s) * kDF + l0 + ll;
                    tile[s][ll] = (unsigned)w_r[idx] | ((unsigned)w_i[idx] << 16);
                }
            }
            __syncthreads();
            const int ss = tid & 63, lr = tid >> 6;
#pragma unroll
            for (int i = 0; i < 16; ++i) {
                const int l = lr + 4 * i;
                if (l0 + l < kDF) WT[(size_t)(l0 + l) * kS + s0 + ss] = tile[ss][l];
            }
            return;
        }
        b -= kNTW;
        {
            const int idx = b * 256 + tid;
            if (idx < 2048) {
                float s, c;
                sincospif(-2.0f * (float)idx / 2048.0f, &s, &c);
                tw2048_g[idx] = make_float2(c, s);
            } else if (idx < 2048 + 640) {
                float s, c;
                sincospif(-2.0f * (float)(idx - 2048) / 640.0f, &s, &c);
                tw640_g[idx - 2048] = make_float2(c, s);
            }
            return;
        }
    }
    const int row0 = bid * kRowsWG;
    STAMP(1, 0);
    const int r = tid >> 5, l32 = tid & 31;
    const bf16_t* xr = x + (size_t)(row0 + r) * kD;
    uint2 xv[5];
#pragma unroll
    for (int i = 0; i < 5; ++i) xv[i] = *reinterpret_cast<const uint2*>(xr + 4 * (l32 + 32 * i));
    for (int i = tid; i < 640; i += 256) {
        float s, c;
        sincospif(-2.0f * (float)i / 640.0f, &s, &c);
        tw[i] = make_float2(c, s);
    }
    float v[20];
    float sum = 0.f;
#pragma unroll
    for (int i = 0; i < 5; ++i) {
        v[4 * i + 0] = bf2f((bf16_t)(xv[i].x & 0xffffu)); v[4 * i + 1] = bf2f((bf16_t)(xv[i].x >> 16));
        v[4 * i + 2] = bf2f((bf16_t)(xv[i].y & 0xffffu)); v[4 * i + 3] = bf2f((bf16_t)(xv[i].y >> 16));
        sum += (v[4 * i] + v[4 * i + 1]) + (v[4 * i + 2] + v[4 * i + 3]);
    }
#pragma unroll
    for (int off = 16; off > 0; off >>= 1) sum += __shfl_xor(sum, off);
    const float mean = sum * (1.0f / kD);
    const float mb = rbf(mean);
    float ss = 0.f;
#pragma unroll
    for (int i = 0; i < 20; ++i) { float d = v[i] - mean; ss += d * d; }
#pragma unroll
    for (int off = 16; off > 0; off >>= 1) ss += __shfl_xor(ss, off);
    const float var = ss * (1.0f / kD);
    const float den = rbf(sqrtf(rbf(rbf(var) + kLnEps)));
    {
        float* bufr = reinterpret_cast<float*>(buf[0]);
#pragma unroll
        for (int i = 0; i < 5; ++i) {
            float4 o;
            o.x = rbf(rbf(v[4 * i + 0] - mb) / den);
            o.y = rbf(rbf(v[4 * i + 1] - mb) / den);
            o.z = rbf(rbf(v[4 * i + 2] - mb) / den);
            o.w = rbf(rbf(v[4 * i + 3] - mb) / den);
            *reinterpret_cast<float4*>(bufr + r * kD + 4 * (l32 + 32 * i)) = o;
        }
    }
    __syncthreads();
    STAMP(1, 1);
    stockham_pass<8, 320, 320, 1, kRowsWG, 2, false>(buf[0], buf[1], tw, tid, 256);
    __syncthreads();
    STAMP(1, 2);
    stockham_pass<8, 320, 40, 8, kRowsWG, 16, false>(buf[1], buf[0], tw, tid, 256);
    __syncthreads();
    STAMP(1, 3);
    stockham_pass<5, 320, 5, 64, kRowsWG, 128, false>(buf[0], buf[1], tw, tid, 256);
    __syncthreads();
    STAMP(1, 4);
    const float2* Z = buf[1];
    for (int k = tid; k < kDF; k += 256) {
        const int km = (320 - k) % 320;
        const int kk = (k == 320) ? 0 : k;
        const float2 w = tw[k];
        float2 outv[kRowsWG];
#pragma unroll
        for (int rr = 0; rr < kRowsWG; ++rr) {
            const float2 Ze = Z[rr * 320 + kk];
            const float2 Zm = Z[rr * 320 + km];
            const float2 E = make_float2(0.5f * (Ze.x + Zm.x), 0.5f * (Ze.y - Zm.y));
            const float dx = Ze.x - Zm.x, dy = Ze.y + Zm.y;
            const float2 O = make_float2(0.5f * dy, -0.5f * dx);
            outv[rr] = cadd(E, cmul(w, O));
        }
        float4* dst = reinterpret_cast<float4*>(T + (size_t)k * kS + row0);
#pragma unroll
        for (int j = 0; j < kRowsWG / 2; ++j)
            dst[j] = make_float4(outv[2 * j].x, outv[2 * j].y, outv[2 * j + 1].x, outv[2 * j + 1].y);
    }
    STAMP(1, 5);
}

__global__ void __launch_bounds__(256) colfft_kernel(const float2* __restrict__ T,
                                                     float2* __restrict__ T2,
                                                     const unsigned* __restrict__ WT,
                                                     const float2* __restrict__ tw2048_g) {
    __shared__ __attribute__((aligned(16))) float2 tw[2048];
    __shared__ __attribute__((aligned(16))) float2 buf[2][2048];
    const int tid = threadIdx.x;
    const int l = blockIdx.x;
    STAMP(2, 0);
    unsigned wp[8];
#pragma unroll
    for (int j = 0; j < 8; ++j) wp[j] = WT[(size_t)l * kS + tid + 256 * j];
    const float4* tw4 = reinterpret_cast<const float4*>(tw2048_g);
    float4* twd = reinterpret_cast<float4*>(tw);
    const float4* col4 = reinterpret_cast<const float4*>(T + (size_t)l * kS);
    float4* b04 = reinterpret_cast<float4*>(buf[0]);
    float4 tv[4], cv[4];
#pragma unroll
    for (int i = 0; i < 4; ++i) { tv[i] = tw4[tid + 256 * i]; cv[i] = col4[tid + 256 * i]; }
#pragma unroll
    for (int i = 0; i < 4; ++i) { twd[tid + 256 * i] = tv[i]; b04[tid + 256 * i] = cv[i]; }
    __syncthreads();
    STAMP(2, 1);
    stockham_pass<8, 2048, 2048, 1, 1, 1, false>(buf[0], buf[1], tw, tid, 256);
    __syncthreads();
    STAMP(2, 2);
    stockham_pass<8, 2048, 256, 8, 1, 8, false>(buf[1], buf[0], tw, tid, 256);
    __syncthreads();
    STAMP(2, 3);
    stockham_pass<8, 2048, 32, 64, 1, 64, false>(buf[0], buf[1], tw, tid, 256);
    __syncthreads();
    STAMP(2, 4);
    {
        const float sc = 1.0f / 2048.0f;
#pragma unroll
        for (int t2 = 0; t2 < 2; ++t2) {
            const int q = tid + 256 * t2;
            float2 a[4];
#pragma unroll
            for (int k = 0; k < 4; ++k) a[k] = buf[1][512 * k + q];
            dft4<false>(a[0], a[1], a[2], a[3]);
#pragma unroll
            for (int k = 0; k < 4; ++k) {
                const int j = 2 * k + t2;
                const float2 wc = make_float2(bf2f((bf16_t)(wp[j] & 0xffffu)) * sc, bf2f((bf16_t)(wp[j] >> 16)) * sc);
                buf[0][512 * k + q] = cmul(a[k], wc);
            }
        }
    }
    __syncthreads();
    STAMP(2, 5);
    stockham_pass<8, 2048, 2048, 1, 1, 1, true>(buf[0], buf[1], tw, tid, 256);
    __syncthreads();
    STAMP(2, 6);
    stockham_pass<8, 2048, 256, 8, 1, 8, true>(buf[1], buf[0], tw, tid, 256);
    __syncthreads();
    STAMP(2, 7);
    stockham_pass<8, 2048, 32, 64, 1, 64, true>(buf[0], buf[1], tw, tid, 256);
    __syncthreads();
    STAMP(2, 8);
#pragma unroll
    for (int t2 = 0; t2 < 2; ++t2) {
        const int q = tid + 256 * t2;
        float2 a[4];
#pragma unroll
        for (int k = 0; k < 4; ++k) a[k] = buf[1][512 * k + q];
        dft4<true>(a[0], a[1], a[2], a[3]);
#pragma unroll
        for (int k = 0; k < 4; ++k) T2[(size_t)l * kS + 512 * k + q] = a[k];
    }
    STAMP(2, 9);
}

__global__ void __launch_bounds__(256) irfft_ln_kernel(const bf16_t* __restrict__ x,
                                                       const float2* __restrict__ T2,
                                                       const float2* __restrict__ tw640_g,
                                                       bf16_t* __restrict__ hout,
                                                       bf16_t* __restrict__ yln2) {
    __shared__ float2 tw[640];
    __shared__ __attribute__((aligned(16))) float2 X[kRowsWG][322];
    __shared__ __attribute__((aligned(16))) float2 buf[2][kRowsWG * 320];
    const int tid = threadIdx.x;
    const int row0 = blockIdx.x * kRowsWG;
    STAMP(3, 0);
    const int r = tid >> 5, l32 = tid & 31;
    const bf16_t* xrow = x + (size_t)(row0 + r) * kD;
    uint2 xv[5];
#pragma unroll
    for (int i = 0; i < 5; ++i) xv[i] = *reinterpret_cast<const uint2*>(xrow + 4 * (l32 + 32 * i));
    for (int i = tid; i < 640; i += 256) tw[i] = tw640_g[i];
    for (int k = tid; k < kDF; k += 256) {
        const float4* src = reinterpret_cast<const float4*>(T2 + (size_t)k * kS + row0);
        float4 v0 = src[0], v1 = src[1], v2 = src[2], v3 = src[3];
        X[0][k] = make_float2(v0.x, v0.y); X[1][k] = make_float2(v0.z, v0.w);
        X[2][k] = make_float2(v1.x, v1.y); X[3][k] = make_float2(v1.z, v1.w);
        X[4][k] = make_float2(v2.x, v2.y); X[5][k] = make_float2(v2.z, v2.w);
        X[6][k] = make_float2(v3.x, v3.y); X[7][k] = make_float2(v3.z, v3.w);
    }
    __syncthreads();
    STAMP(3, 1);
    for (int task = tid; task < kRowsWG * 320; task += 256) {
        const int rr = task / 320, k = task - rr * 320;
        float2 Xk = X[rr][k];
        float2 Xm = X[rr][320 - k];
        if (k == 0) { Xk.y = 0.f; Xm.y = 0.f; }
        const float2 E = make_float2(0.5f * (Xk.x + Xm.x), 0.5f * (Xk.y - Xm.y));
        const float2 d = make_float2(Xk.x - Xm.x, Xk.y + Xm.y);
        float2 O = cmulc(d, tw[k]);
        O.x *= 0.5f; O.y *= 0.5f;
        buf[0][rr * 320 + k] = make_float2(E.x - O.y, E.y + O.x);
    }
    __syncthreads();
    STAMP(3, 2);
    stockham_pass<8, 320, 320, 1, kRowsWG, 2, true>(buf[0], buf[1], tw, tid, 256);
    __syncthreads();
    STAMP(3, 3);
    stockham_pass<8, 320, 40, 8, kRowsWG, 16, true>(buf[1], buf[0], tw, tid, 256);
    __syncthreads();
    STAMP(3, 4);
    stockham_pass<5, 320, 5, 64, kRowsWG, 128, true>(buf[0], buf[1], tw, tid, 256);
    __syncthreads();
    STAMP(3, 5);
    const float* zr = reinterpret_cast<const float*>(buf[1] + r * 320);
    bf16_t* hrow = hout + (size_t)(row0 + r) * kD;
    bf16_t* yrow = yln2 + (size_t)(row0 + r) * kD;
    float hv[20];
    float sum = 0.f;
#pragma unroll
    for (int i = 0; i < 5; ++i) {
        const int c = l32 + 32 * i;
        const float4 zv = *reinterpret_cast<const float4*>(zr + 4 * c);
        const float xa = bf2f((bf16_t)(xv[i].x & 0xffffu)), xb = bf2f((bf16_t)(xv[i].x >> 16));
        const float xc = bf2f((bf16_t)(xv[i].y & 0xffffu)), xd = bf2f((bf16_t)(xv[i].y >> 16));
        const float h0 = rbf(xa + rbf(zv.x * (1.0f / 320.0f)));
        const float h1 = rbf(xb + rbf(zv.y * (1.0f / 320.0f)));
        const float h2 = rbf(xc + rbf(zv.z * (1.0f / 320.0f)));
        const float h3 = rbf(xd + rbf(zv.w * (1.0f / 320.0f)));
        hv[4 * i] = h0; hv[4 * i + 1] = h1; hv[4 * i + 2] = h2; hv[4 * i + 3] = h3;
        uint2 hp;
        hp.x = (unsigned)f2bf(h0) | ((unsigned)f2bf(h1) << 16);
        hp.y = (unsigned)f2bf(h2) | ((unsigned)f2bf(h3) << 16);
        *reinterpret_cast<uint2*>(hrow + 4 * c) = hp;
        sum += (h0 + h1) + (h2 + h3);
    }
#pragma unroll
    for (int off = 16; off > 0; off >>= 1) sum += __shfl_xor(sum, off);
    const float mean = sum * (1.0f / kD);
    const float mb = rbf(mean);
    float ss = 0.f;
#pragma unroll
    for (int i = 0; i < 20; ++i) { float d = hv[i] - mean; ss += d * d; }
#pragma unroll
    for (int off = 16; off > 0; off >>= 1) ss += __shfl_xor(ss, off);
    const float var = ss * (1.0f / kD);
    const float den = rbf(sqrtf(rbf(rbf(var) + kLnEps)));
    float y1[20];
    float sum2 = 0.f;
#pragma unroll
    for (int i = 0; i < 20; ++i) { y1[i] = rbf(rbf(hv[i] - mb) / den); sum2 += y1[i]; }
#pragma unroll
    for (int off = 16; off > 0; off >>= 1) sum2 += __shfl_xor(sum2, off);
    const float mean2 = sum2 * (1.0f / kD);
    const float mb2 = rbf(mean2);
    float ss2 = 0.f;
#pragma unroll
    for (int i = 0; i < 20; ++i) { float d = y1[i] - mean2; ss2 += d * d; }
#pragma unroll
    for (int off = 16; off > 0; off >>= 1) ss2 += __shfl_xor(ss2, off);
    const float var2 = ss2 * (1.0f / kD);
    const float den2 = rbf(sqrtf(rbf(rbf(var2) + kLnEps)));
#pragma unroll
    for (int i = 0; i < 5; ++i) {
        const int c = l32 + 32 * i;
        uint2 yp;
        yp.x = (unsigned)f2bf(rbf(y1[4 * i] - mb2) / den2) | ((unsigned)f2bf(rbf(y1[4 * i + 1] - mb2) / den2) << 16);
        yp.y = (unsigned)f2bf(rbf(y1[4 * i + 2] - mb2) / den2) | ((unsigned)f2bf(rbf(y1[4 * i + 3] - mb2) / den2) << 16);
        *reinterpret_cast<uint2*>(yrow + 4 * c) = yp;
    }
    STAMP(3, 6);
}

using bf16x8 = __attribute__((ext_vector_type(8))) __bf16;
using f32x16 = __attribute__((ext_vector_type(16))) float;
typedef unsigned __attribute__((ext_vector_type(4))) u32x4;
typedef unsigned __attribute__((ext_vector_type(2))) u32x2;

#define WAIT_V(n) asm volatile("s_waitcnt vmcnt(%0)" ::"n"(n) : "memory")
#define WAIT_L(n) asm volatile("s_waitcnt lgkmcnt(%0)" ::"n"(n) : "memory")
#define RAW_BARRIER() do { WAIT_L(0); __builtin_amdgcn_s_barrier(); } while (0)

__device__ __forceinline__ float gelu_tanh_f(float t) {
    const float x2 = t * __fmaf_rn(0.0713548162726008f, t * t, 1.5957691216057308f);
    return t * __frcp_rn(1.0f + __expf(-x2));
}

template <int TM, int TN, int WM, int WN, int NBUF, int EPI>
__global__ void __launch_bounds__(256) gemm_kernel(const bf16_t* __restrict__ A, int lda,
                                                   const bf16_t* __restrict__ Bt, int ldb, int K,
                                                   const bf16_t* __restrict__ bias,
                                                   const bf16_t* __restrict__ resid,
                                                   bf16_t* __restrict__ C, int ldc) {
    constexpr int BK = 64;
    constexpr int D = NBUF - 1;
    constexpr int WTM = TM / WM, WTN = TN / WN;
    constexpr int MB = WTM / 32, NB = WTN / 32;
    constexpr int A_TILE_B = TM * 128, B_TILE_B = TN * 128, STAGE_B = A_TILE_B + B_TILE_B;
    constexpr int GA = TM / 32, GB = TN / 32;
    constexpr int GL = GA + GB;
    static_assert(WM * WN == 4, "4 waves");
    static_assert((D - 1) * GL <= 63, "vmcnt range");
    static_assert(4 * WTM * (WTN * 2 + 16) <= NBUF * STAGE_B, "epilogue staging fits");
    __shared__ __attribute__((aligned(1024))) char shm[NBUF * STAGE_B];

    const int tid = threadIdx.x, lane = tid & 63, wave = tid >> 6;
    const int wm = wave / WN, wn = wave % WN;
    const int m0 = blockIdx.y * TM, n0 = blockIdx.x * TN;
    const int r32 = lane & 31, hh = lane >> 5;

    int aoff[GA], boff[GB];
#pragma unroll
    for (int i = 0; i < GA; ++i) {
        const int row = 8 * (wave * GA + i) + (lane >> 3);
        const int c = (lane & 7) ^ ((row >> 1) & 7);
        aoff[i] = (m0 + row) * lda + 8 * c;
    }
#pragma unroll
    for (int i = 0; i < GB; ++i) {
        const int row = 8 * (wave * GB + i) + (lane >> 3);
        const int c = (lane & 7) ^ ((row >> 1) & 7);
        boff[i] = (n0 + row) * ldb + 8 * c;
    }
#define GLDS_STAGE(BUF, KT)                                                                        \
    do {                                                                                           \
        char* sa_ = shm + (BUF) * STAGE_B;                                                         \
        _Pragma("unroll") for (int i = 0; i < GA; ++i)                                             \
            __builtin_amdgcn_global_load_lds((const unsigned*)(A + aoff[i] + (KT) * BK),           \
                                             (unsigned*)(sa_ + (wave * GA + i) * 1024), 16, 0, 0); \
        _Pragma("unroll") for (int i = 0; i < GB; ++i)                                             \
            __builtin_amdgcn_global_load_lds((const unsigned*)(Bt + boff[i] + (KT) * BK),          \
                                             (unsigned*)(sa_ + A_TILE_B + (wave * GB + i) * 1024), 16, 0, 0); \
    } while (0)

    f32x16 acc[MB][NB];
#pragma unroll
    for (int i = 0; i < MB; ++i)
#pragma unroll
        for (int j = 0; j < NB; ++j)
#pragma unroll
            for (int e = 0; e < 16; ++e) acc[i][j][e] = 0.f;

    const int sw = (r32 >> 1) & 7;
    const int KT = K / BK;
    STAMP(4 + EPI, 0);
#pragma unroll
    for (int pt = 0; pt < D; ++pt) GLDS_STAGE(pt, pt);
    WAIT_V((D - 1) * GL);
    RAW_BARRIER();
    STAMP(4 + EPI, 1);
    for (int kt = 0; kt < KT; ++kt) {
        const int cur = kt % NBUF;
        if (kt + D < KT) GLDS_STAGE((kt + D) % NBUF, kt + D);
        const char* sa = shm + cur * STAGE_B;
        const char* sb = sa + A_TILE_B;
#pragma unroll
        for (int kk = 0; kk < 4; ++kk) {
            const int coff = 16 * ((2 * kk + hh) ^ sw);
            bf16x8 af[MB], bfv[NB];
#pragma unroll
            for (int mi = 0; mi < MB; ++mi)
                af[mi] = *reinterpret_cast<const bf16x8*>(sa + (wm * WTM + mi * 32 + r32) * 128 + coff);
#pragma unroll
            for (int ni = 0; ni < NB; ++ni)
                bfv[ni] = *reinterpret_cast<const bf16x8*>(sb + (wn * WTN + ni * 32 + r32) * 128 + coff);
#pragma unroll
            for (int mi = 0; mi < MB; ++mi)
#pragma unroll
                for (int ni = 0; ni < NB; ++ni)
                    acc[mi][ni] = __builtin_amdgcn_mfma_f32_32x32x16_bf16(bfv[ni], af[mi], acc[mi][ni], 0, 0, 0);
        }
        if (kt + D < KT) WAIT_V((D - 1) * GL); else WAIT_V(0);
        RAW_BARRIER();
    }
    STAMP(4 + EPI, 2);
#undef GLDS_STAGE
    constexpr int SSTR = WTN * 2 + 16;
    char* stg = shm + wave * (WTM * SSTR);
#pragma unroll
    for (int ni = 0; ni < NB; ++ni) {
#pragma unroll
        for (int g = 0; g < 4; ++g) {
            const int nl = ni * 32 + 8 * g + 4 * hh;
            const u32x2 bb = *reinterpret_cast<const u32x2*>(bias + n0 + wn * WTN + nl);
            const float b0 = bf2f((bf16_t)(bb[0] & 0xffffu)), b1 = bf2f((bf16_t)(bb[0] >> 16));
            const float b2 = bf2f((bf16_t)(bb[1] & 0xffffu)), b3 = bf2f((bf16_t)(bb[1] >> 16));
#pragma unroll
            for (int mi = 0; mi < MB; ++mi) {
                float t0 = rbf(rbf(acc[mi][ni][4 * g + 0]) + b0);
                float t1 = rbf(rbf(acc[mi][ni][4 * g + 1]) + b1);
                float t2 = rbf(rbf(acc[mi][ni][4 * g + 2]) + b2);
                float t3 = rbf(rbf(acc[mi][ni][4 * g + 3]) + b3);
                if (EPI == 0) { t0 = gelu_tanh_f(t0); t1 = gelu_tanh_f(t1); t2 = gelu_tanh_f(t2); t3 = gelu_tanh_f(t3); }
                u32x2 pk;
                pk[0] = (unsigned)f2bf(t0) | ((unsigned)f2bf(t1) << 16);
                pk[1] = (unsigned)f2bf(t2) | ((unsigned)f2bf(t3) << 16);
                *reinterpret_cast<u32x2*>(stg + (mi * 32 + r32) * SSTR + nl * 2) = pk;
            }
        }
    }
    WAIT_L(0);
    constexpr int CPR = WTN / 8;
    constexpr int NU = WTM * CPR / 64;
#pragma unroll
    for (int i = 0; i < NU; ++i) {
        const int u = lane + 64 * i;
        const int row = u / CPR, ch = u % CPR;
        const u32x4 v = *reinterpret_cast<const u32x4*>(stg + row * SSTR + 16 * ch);
        const size_t go = (size_t)(m0 + wm * WTM + row) * ldc + n0 + wn * WTN + 8 * ch;
        if (EPI == 0) {
            *reinterpret_cast<u32x4*>(C + go) = v;
        } else {
            const u32x4 hr = *reinterpret_cast<const u32x4*>(resid + go);
            u32x4 o;
#pragma unroll
            for (int q = 0; q < 4; ++q) {
                const float lo = bf2f((bf16_t)(hr[q] & 0xffffu)) + bf2f((bf16_t)(v[q] & 0xffffu));
                const float hi = bf2f((bf16_t)(hr[q] >> 16)) + bf2f((bf16_t)(v[q] >> 16));
                o[q] = (unsigned)f2bf(lo) | ((unsigned)f2bf(hi) << 16);
            }
            *reinterpret_cast<u32x4*>(C + go) = o;
        }
    }
    STAMP(4 + EPI, 3);
}

extern "C" void kernel_launch(void* const* d_in, const int* in_sizes, int n_in,
                              void* d_out, int out_size, void* d_ws, size_t ws_size,
                              hipStream_t stream) {
    (void)in_sizes; (void)n_in; (void)out_size; (void)ws_size;
    const bf16_t* A_x  = (const bf16_t*)d_in[0];
    const bf16_t* A_wr = (const bf16_t*)d_in[1];
    const bf16_t* A_wi = (const bf16_t*)d_in[2];
    const bf16_t* B_w1 = (const bf16_t*)d_in[4];
    const bf16_t* B_b1 = (const bf16_t*)d_in[5];
    const bf16_t* B_w2 = (const bf16_t*)d_in[6];
    const bf16_t* B_b2 = (const bf16_t*)d_in[7];
    bf16_t* out = (bf16_t*)d_out;

    char* ws = (char*)d_ws;
    float2* T      = (float2*)(ws + OFF_T);
    float2* T2     = (float2*)(ws + OFF_T2);
    bf16_t* hbuf   = (bf16_t*)(ws + OFF_H);
    bf16_t* yln2   = (bf16_t*)(ws + OFF_YLN2);
    bf16_t* Hbuf   = (bf16_t*)(ws + OFF_G);
    bf16_t* W1t    = (bf16_t*)(ws + OFF_W1T);
    bf16_t* W2t    = (bf16_t*)(ws + OFF_W2T);
    float2* tw2048 = (float2*)(ws + OFF_TW2048);
    float2* tw640  = (float2*)(ws + OFF_TW640);
    unsigned* WT   = (unsigned*)(ws + OFF_WT);

    ln_rfft_kernel<<<kNBlkA, 256, 0, stream>>>(A_x, T, B_w1, B_w2, W1t, W2t, A_wr, A_wi, WT, tw2048, tw640);
    colfft_kernel<<<kDF, 256, 0, stream>>>(T, T2, WT, tw2048);
    irfft_ln_kernel<<<kS / kRowsWG, 256, 0, stream>>>(A_x, T2, tw640, hbuf, yln2);
    gemm_kernel<256, 64, 4, 1, 3, 0><<<dim3(kF / 64, kS / 256), 256, 0, stream>>>(
        yln2, kD, W1t, kD, kD, B_b1, nullptr, Hbuf, kF);
    gemm_kernel<128, 64, 2, 2, 5, 1><<<dim3(kD / 64, kS / 128), 256, 0, stream>>>(
        Hbuf, kF, W2t, kF, kF, B_b2, hbuf, out, kD);
}
```

```cpp
#include <hip/hip_runtime.h>
#include <hip/hip_bf16.h>
#include <stdint.h>

static constexpr int kS  = 2048;
static constexpr int kD  = 640;
static constexpr int kDF = 321;
static constexpr int kF  = 1728;
static constexpr float kLnEps = 1e-5f;

static constexpr size_t OFF_T     = 0;
static constexpr size_t OFF_T2    = OFF_T   + (size_t)kDF * kS * 8;
static constexpr size_t OFF_H     = OFF_T2  + (size_t)kDF * kS * 8;
static constexpr size_t OFF_YLN2  = OFF_H   + (size_t)kS * kD * 2;
static constexpr size_t OFF_G     = OFF_YLN2+ (size_t)kS * kD * 2;
static constexpr size_t OFF_W1T   = OFF_G   + (size_t)kS * kF * 2;
static constexpr size_t OFF_W2T   = OFF_W1T + (size_t)kF * kD * 2;
static constexpr size_t OFF_TW2048= OFF_W2T + (size_t)kD * kF * 2;
static constexpr size_t OFF_TW640 = OFF_TW2048 + (size_t)2048 * 8;
static constexpr size_t OFF_WT    = OFF_TW640 + (size_t)640 * 8;

typedef unsigned short bf16_t;

#define STAMP(KID, I) do { } while (0)

__device__ __forceinline__ float bf2f(bf16_t u) {
    return __uint_as_float(((unsigned)u) << 16);
}
__device__ __forceinline__ bf16_t f2bf(float f) {
    return __builtin_bit_cast(bf16_t, (__bf16)f);
}
typedef __attribute__((ext_vector_type(2))) __bf16 bf16x2_t;
typedef __attribute__((ext_vector_type(2))) float f32x2_t;
__device__ __forceinline__ unsigned pack2bf(float lo, float hi) {
    f32x2_t v = {lo, hi};
    bf16x2_t b = __builtin_convertvector(v, bf16x2_t);
    return __builtin_bit_cast(unsigned, b);
}
__device__ __forceinline__ float rbf(float f) { return bf2f(f2bf(f)); }

__device__ __forceinline__ float2 cadd(float2 a, float2 b) { return make_float2(a.x + b.x, a.y + b.y); }
__device__ __forceinline__ float2 csub(float2 a, float2 b) { return make_float2(a.x - b.x, a.y - b.y); }
__device__ __forceinline__ float2 cmul(float2 a, float2 b) {
    return make_float2(__fmaf_rn(a.x, b.x, -a.y * b.y), __fmaf_rn(a.x, b.y, a.y * b.x));
}
__device__ __forceinline__ float2 cmulc(float2 a, float2 b) {
    return make_float2(__fmaf_rn(a.x, b.x, a.y * b.y), __fmaf_rn(a.y, b.x, -a.x * b.y));
}
template <bool INV>
__device__ __forceinline__ float2 cmul_j(float2 a) {
    return INV ? make_float2(-a.y, a.x) : make_float2(a.y, -a.x);
}

template <bool INV>
__device__ __forceinline__ void dft4(float2 &b0, float2 &b1, float2 &b2, float2 &b3) {
    float2 t0 = cadd(b0, b2), t1 = csub(b0, b2), t2 = cadd(b1, b3), t3 = cmul_j<INV>(csub(b1, b3));
    b0 = cadd(t0, t2); b2 = csub(t0, t2); b1 = cadd(t1, t3); b3 = csub(t1, t3);
}
template <bool INV>
__device__ __forceinline__ void dft5(float2 a[5]) {
    const float c1 = 0.30901699437494745f, c2 = -0.8090169943749473f;
    const float s1 = 0.9510565162951535f,  s2 = 0.5877852522924731f;
    float2 b1 = cadd(a[1], a[4]), b4 = csub(a[1], a[4]);
    float2 b2 = cadd(a[2], a[3]), b3 = csub(a[2], a[3]);
    float2 t1 = make_float2(a[0].x + c1 * b1.x + c2 * b2.x, a[0].y + c1 * b1.y + c2 * b2.y);
    float2 t2 = make_float2(a[0].x + c2 * b1.x + c1 * b2.x, a[0].y + c2 * b1.y + c1 * b2.y);
    float2 u1 = make_float2(s1 * b4.x + s2 * b3.x, s1 * b4.y + s2 * b3.y);
    float2 u2 = make_float2(s2 * b4.x - s1 * b3.x, s2 * b4.y - s1 * b3.y);
    float2 iu1 = cmul_j<INV>(u1), iu2 = cmul_j<INV>(u2);
    a[0] = make_float2(a[0].x + b1.x + b2.x, a[0].y + b1.y + b2.y);
    a[1] = cadd(t1, iu1); a[4] = csub(t1, iu1);
    a[2] = cadd(t2, iu2); a[3] = csub(t2, iu2);
}
template <bool INV>
__device__ __forceinline__ void dft8(float2 a[8]) {
    const float r2 = 0.70710678118654752f;
    float2 e0 = a[0], e1 = a[2], e2 = a[4], e3 = a[6];
    float2 o0 = a[1], o1 = a[3], o2 = a[5], o3 = a[7];
    dft4<INV>(e0, e1, e2, e3);
    dft4<INV>(o0, o1, o2, o3);
    float2 p1, p3;
    if (!INV) {
        p1 = make_float2((o1.x + o1.y) * r2, (o1.y - o1.x) * r2);
        p3 = make_float2((o3.y - o3.x) * r2, -(o3.x + o3.y) * r2);
    } else {
        p1 = make_float2((o1.x - o1.y) * r2, (o1.x + o1.y) * r2);
        p3 = make_float2(-(o3.x + o3.y) * r2, (o3.x - o3.y) * r2);
    }
    float2 p2 = cmul_j<INV>(o2);
    a[0] = cadd(e0, o0); a[4] = csub(e0, o0);
    a[1] = cadd(e1, p1); a[5] = csub(e1, p1);
    a[2] = cadd(e2, p2); a[6] = csub(e2, p2);
    a[3] = cadd(e3, p3); a[7] = csub(e3, p3);
}

template <int R, bool INV> struct Dft;
template <bool INV> struct Dft<4, INV> {
    __device__ __forceinline__ static void run(float2 a[4]) { dft4<INV>(a[0], a[1], a[2], a[3]); }
};
template <bool INV> struct Dft<5, INV> {
    __device__ __forceinline__ static void run(float2 a[5]) { dft5<INV>(a); }
};
template <bool INV> struct Dft<8, INV> {
    __device__ __forceinline__ static void run(float2 a[8]) { dft8<INV>(a); }
};

template <int R, int NT, int NN, int SS, int NROWS, int TWSCALE, bool INV>
__device__ __forceinline__ void stockham_pass(const float2* __restrict__ in, float2* __restrict__ out,
                                              const float2* __restrict__ tw, int tid, int nthreads) {
    constexpr int M = NN / R;
    constexpr int NB = NT / R;
    for (int bt = tid; bt < NROWS * NB; bt += nthreads) {
        const int r   = bt / NB;
        const int rem = bt - r * NB;
        const int p   = rem / SS;
        const int q   = rem - p * SS;
        const float2* rin = in + r * NT;
        float2* rout = out + r * NT;
        float2 a[R];
#pragma unroll
        for (int k = 0; k < R; ++k) a[k] = rin[SS * (p + k * M) + q];
        Dft<R, INV>::run(a);
        if (NN != R) {
#pragma unroll
            for (int k = 1; k < R; ++k) {
                float2 w = tw[p * k * TWSCALE];
                a[k] = INV ? cmulc(a[k], w) : cmul(a[k], w);
            }
        }
#pragma unroll
        for (int k = 0; k < R; ++k) rout[SS * (R * p + k) + q] = a[k];
    }
}

static constexpr int kRowsWG = 8;
static constexpr int kNBlkFFT = kS / kRowsWG;
static constexpr int kNT1 = (kD / 64) * (kF / 64);
static constexpr int kNT2 = (kF / 64) * (kD / 64);
static constexpr int kNTW = (kS / 64) * ((kDF + 63) / 64);
static constexpr int kNTB = (2048 + 640 + 255) / 256;
static constexpr int kNBlkA = kNBlkFFT + kNT1 + kNT2 + kNTW + kNTB;

__device__ __forceinline__ void transpose_tile64(const bf16_t* __restrict__ src, bf16_t* __restrict__ dst,
                                                 int R, int C, int r0, int c0, bf16_t (*tile)[66], int tid) {
#pragma unroll
    for (int i = 0; i < 2; ++i) {
        int u = tid + 256 * i;
        int row = u >> 3, ch = u & 7;
        uint4 v = *reinterpret_cast<const uint4*>(src + (size_t)(r0 + row) * C + c0 + 8 * ch);
        const bf16_t* e = reinterpret_cast<const bf16_t*>(&v);
#pragma unroll
        for (int j = 0; j < 8; ++j) tile[row][8 * ch + j] = e[j];
    }
    __syncthreads();
#pragma unroll
    for (int i = 0; i < 2; ++i) {
        int u = tid + 256 * i;
        int orow = u >> 3, ch = u & 7;
        uint4 v;
        bf16_t* e = reinterpret_cast<bf16_t*>(&v);
#pragma unroll
        for (int j = 0; j < 8; ++j) e[j] = tile[8 * ch + j][orow];
        *reinterpret_cast<uint4*>(dst + (size_t)(c0 + orow) * R + r0 + 8 * ch) = v;
    }
}

__global__ void __launch_bounds__(256) ln_rfft_kernel(const bf16_t* __restrict__ x,
                                                      float2* __restrict__ T,
                                                      const bf16_t* __restrict__ W1, const bf16_t* __restrict__ W2,
                                                      bf16_t* __restrict__ W1t, bf16_t* __restrict__ W2t,
                                                      const bf16_t* __restrict__ w_r, const bf16_t* __restrict__ w_i,
                                                      unsigned* __restrict__ WT,
                                                      float2* __restrict__ tw2048_g, float2* __restrict__ tw640_g) {
    __shared__ float2 tw[640];
    __shared__ __attribute__((aligned(16))) float2 buf[2][kRowsWG * 320];
    const int tid = threadIdx.x;
    const int bid = blockIdx.x;
    if (bid >= kNBlkFFT) {
        int b = bid - kNBlkFFT;
        if (b < kNT1) {
            bf16_t (*tile)[66] = reinterpret_cast<bf16_t (*)[66]>(&buf[0][0]);
            transpose_tile64(W1, W1t, kD, kF, (b / (kF / 64)) * 64, (b % (kF / 64)) * 64, tile, tid);
            return;
        }
        b -= kNT1;
        if (b < kNT2) {
            bf16_t (*tile)[66] = reinterpret_cast<bf16_t (*)[66]>(&buf[0][0]);
            transpose_tile64(W2, W2t, kF, kD, (b / (kD / 64)) * 64, (b % (kD / 64)) * 64, tile, tid);
            return;
        }
        b -= kNT2;
        if (b < kNTW) {
            unsigned (*tile)[65] = reinterpret_cast<unsigned (*)[65]>(&buf[0][0]);
            const int s0 = (b / 6) * 64, l0 = (b % 6) * 64;
            const int ll = tid & 63, sr = tid >> 6;
            if (l0 + ll < kDF) {
#pragma unroll
                for (int i = 0; i < 16; ++i) {
                    const int s = sr + 4 * i;
                    const size_t idx = (size_t)(s0 + s) * kDF + l0 + ll;
                    tile[s][ll] = (unsigned)w_r[idx] | ((unsigned)w_i[idx] << 16);
                }
            }
            __syncthreads();
            const int ss = tid & 63, lr = tid >> 6;
#pragma unroll
            for (int i = 0; i < 16; ++i) {
                const int l = lr + 4 * i;
                if (l0 + l < kDF) WT[(size_t)(l0 + l) * kS + s0 + ss] = tile[ss][l];
            }
            return;
        }
        b -= kNTW;
        {
            const int idx = b * 256 + tid;
            if (idx < 2048) {
                float s, c;
                sincospif(-2.0f * (float)idx / 2048.0f, &s, &c);
                tw2048_g[idx] = make_float2(c, s);
            } else if (idx < 2048 + 640) {
                float s, c;
                sincospif(-2.0f * (float)(idx - 2048) / 640.0f, &s, &c);
                tw640_g[idx - 2048] = make_float2(c, s);
            }
            return;
        }
    }
    const int row0 = bid * kRowsWG;
    STAMP(1, 0);
    const int r = tid >> 5, l32 = tid & 31;
    const bf16_t* xr = x + (size_t)(row0 + r) * kD;
    uint2 xv[5];
#pragma unroll
    for (int i = 0; i < 5; ++i) xv[i] = *reinterpret_cast<const uint2*>(xr + 4 * (l32 + 32 * i));
    for (int i = tid; i < 640; i += 256) {
        float s, c;
        sincospif(-2.0f * (float)i / 640.0f, &s, &c);
        tw[i] = make_float2(c, s);
    }
    float v[20];
    float sum = 0.f;
#pragma unroll
    for (int i = 0; i < 5; ++i) {
        v[4 * i + 0] = bf2f((bf16_t)(xv[i].x & 0xffffu)); v[4 * i + 1] = bf2f((bf16_t)(xv[i].x >> 16));
        v[4 * i + 2] = bf2f((bf16_t)(xv[i].y & 0xffffu)); v[4 * i + 3] = bf2f((bf16_t)(xv[i].y >> 16));
        sum += (v[4 * i] + v[4 * i + 1]) + (v[4 * i + 2] + v[4 * i + 3]);
    }
#pragma unroll
    for (int off = 16; off > 0; off >>= 1) sum += __shfl_xor(sum, off);
    const float mean = sum * (1.0f / kD);
    const float mb = rbf(mean);
    float ss = 0.f;
#pragma unroll
    for (int i = 0; i < 20; ++i) { float d = v[i] - mean; ss += d * d; }
#pragma unroll
    for (int off = 16; off > 0; off >>= 1) ss += __shfl_xor(ss, off);
    const float var = ss * (1.0f / kD);
    const float den = rbf(sqrtf(rbf(rbf(var) + kLnEps)));
    {
        float* bufr = reinterpret_cast<float*>(buf[0]);
#pragma unroll
        for (int i = 0; i < 5; ++i) {
            float4 o;
            o.x = rbf(rbf(v[4 * i + 0] - mb) / den);
            o.y = rbf(rbf(v[4 * i + 1] - mb) / den);
            o.z = rbf(rbf(v[4 * i + 2] - mb) / den);
            o.w = rbf(rbf(v[4 * i + 3] - mb) / den);
            *reinterpret_cast<float4*>(bufr + r * kD + 4 * (l32 + 32 * i)) = o;
        }
    }
    __syncthreads();
    STAMP(1, 1);
    stockham_pass<8, 320, 320, 1, kRowsWG, 2, false>(buf[0], buf[1], tw, tid, 256);
    __syncthreads();
    STAMP(1, 2);
    stockham_pass<8, 320, 40, 8, kRowsWG, 16, false>(buf[1], buf[0], tw, tid, 256);
    __syncthreads();
    STAMP(1, 3);
    stockham_pass<5, 320, 5, 64, kRowsWG, 128, false>(buf[0], buf[1], tw, tid, 256);
    __syncthreads();
    STAMP(1, 4);
    const float2* Z = buf[1];
    for (int k = tid; k < kDF; k += 256) {
        const int km = (320 - k) % 320;
        const int kk = (k == 320) ? 0 : k;
        const float2 w = tw[k];
        float2 outv[kRowsWG];
#pragma unroll
        for (int rr = 0; rr < kRowsWG; ++rr) {
            const float2 Ze = Z[rr * 320 + kk];
            const float2 Zm = Z[rr * 320 + km];
            const float2 E = make_float2(0.5f * (Ze.x + Zm.x), 0.5f * (Ze.y - Zm.y));
            const float dx = Ze.x - Zm.x, dy = Ze.y + Zm.y;
            const float2 O = make_float2(0.5f * dy, -0.5f * dx);
            outv[rr] = cadd(E, cmul(w, O));
        }
        float4* dst = reinterpret_cast<float4*>(T + (size_t)k * kS + row0);
#pragma unroll
        for (int j = 0; j < kRowsWG / 2; ++j)
            dst[j] = make_float4(outv[2 * j].x, outv[2 * j].y, outv[2 * j + 1].x, outv[2 * j + 1].y);
    }
    STAMP(1, 5);
}

__global__ void __launch_bounds__(256) colfft_kernel(const float2* __restrict__ T,
                                                     float2* __restrict__ T2,
                                                     const unsigned* __restrict__ WT,
                                                     const float2* __restrict__ tw2048_g) {
    __shared__ __attribute__((aligned(16))) float2 tw[2048];
    __shared__ __attribute__((aligned(16))) float2 buf[2][2048];
    const int tid = threadIdx.x;
    const int l = blockIdx.x;
    STAMP(2, 0);
    unsigned wp[8];
#pragma unroll
    for (int j = 0; j < 8; ++j) wp[j] = WT[(size_t)l * kS + tid + 256 * j];
    const float4* tw4 = reinterpret_cast<const float4*>(tw2048_g);
    float4* twd = reinterpret_cast<float4*>(tw);
    const float4* col4 = reinterpret_cast<const float4*>(T + (size_t)l * kS);
    float4* b04 = reinterpret_cast<float4*>(buf[0]);
    float4 tv[4], cv[4];
#pragma unroll
    for (int i = 0; i < 4; ++i) { tv[i] = tw4[tid + 256 * i]; cv[i] = col4[tid + 256 * i]; }
#pragma unroll
    for (int i = 0; i < 4; ++i) { twd[tid + 256 * i] = tv[i]; b04[tid + 256 * i] = cv[i]; }
    __syncthreads();
    STAMP(2, 1);
    stockham_pass<8, 2048, 2048, 1, 1, 1, false>(buf[0], buf[1], tw, tid, 256);
    __syncthreads();
    STAMP(2, 2);
    stockham_pass<8, 2048, 256, 8, 1, 8, false>(buf[1], buf[0], tw, tid, 256);
    __syncthreads();
    STAMP(2, 3);
    stockham_pass<8, 2048, 32, 64, 1, 64, false>(buf[0], buf[1], tw, tid, 256);
    __syncthreads();
    STAMP(2, 4);
    {
        const float sc = 1.0f / 2048.0f;
#pragma unroll
        for (int t2 = 0; t2 < 2; ++t2) {
            const int q = tid + 256 * t2;
            float2 a[4];
#pragma unroll
            for (int k = 0; k < 4; ++k) a[k] = buf[1][512 * k + q];
            dft4<false>(a[0], a[1], a[2], a[3]);
#pragma unroll
            for (int k = 0; k < 4; ++k) {
                const int j = 2 * k + t2;
                const float2 wc = make_float2(bf2f((bf16_t)(wp[j] & 0xffffu)) * sc, bf2f((bf16_t)(wp[j] >> 16)) * sc);
                buf[0][512 * k + q] = cmul(a[k], wc);
            }
        }
    }
    __syncthreads();
    STAMP(2, 5);
    stockham_pass<8, 2048, 2048, 1, 1, 1, true>(buf[0], buf[1], tw, tid, 256);
    __syncthreads();
    STAMP(2, 6);
    stockham_pass<8, 2048, 256, 8, 1, 8, true>(buf[1], buf[0], tw, tid, 256);
    __syncthreads();
    STAMP(2, 7);
    stockham_pass<8, 2048, 32, 64, 1, 64, true>(buf[0], buf[1], tw, tid, 256);
    __syncthreads();
    STAMP(2, 8);
#pragma unroll
    for (int t2 = 0; t2 < 2; ++t2) {
        const int q = tid + 256 * t2;
        float2 a[4];
#pragma unroll
        for (int k = 0; k < 4; ++k) a[k] = buf[1][512 * k + q];
        dft4<true>(a[0], a[1], a[2], a[3]);
#pragma unroll
        for (int k = 0; k < 4; ++k) T2[(size_t)l * kS + 512 * k + q] = a[k];
    }
    STAMP(2, 9);
}

__global__ void __launch_bounds__(256) irfft_ln_kernel(const bf16_t* __restrict__ x,
                                                       const float2* __restrict__ T2,
                                                       const float2* __restrict__ tw640_g,
                                                       bf16_t* __restrict__ hout,
                                                       bf16_t* __restrict__ yln2) {
    __shared__ float2 tw[640];
    __shared__ __attribute__((aligned(16))) float2 X[kRowsWG][322];
    __shared__ __attribute__((aligned(16))) float2 buf[2][kRowsWG * 320];
    const int tid = threadIdx.x;
    const int row0 = (((blockIdx.x & 7) * 32) + (blockIdx.x >> 3)) * kRowsWG;
    STAMP(3, 0);
    const int r = tid >> 5, l32 = tid & 31;
    const bf16_t* xrow = x + (size_t)(row0 + r) * kD;
    uint2 xv[5];
#pragma unroll
    for (int i = 0; i < 5; ++i) xv[i] = *reinterpret_cast<const uint2*>(xrow + 4 * (l32 + 32 * i));
    for (int i = tid; i < 640; i += 256) tw[i] = tw640_g[i];
    for (int k = tid; k < kDF; k += 256) {
        const float4* src = reinterpret_cast<const float4*>(T2 + (size_t)k * kS + row0);
        float4 v0 = src[0], v1 = src[1], v2 = src[2], v3 = src[3];
        X[0][k] = make_float2(v0.x, v0.y); X[1][k] = make_float2(v0.z, v0.w);
        X[2][k] = make_float2(v1.x, v1.y); X[3][k] = make_float2(v1.z, v1.w);
        X[4][k] = make_float2(v2.x, v2.y); X[5][k] = make_float2(v2.z, v2.w);
        X[6][k] = make_float2(v3.x, v3.y); X[7][k] = make_float2(v3.z, v3.w);
    }
    __syncthreads();
    STAMP(3, 1);
    for (int task = tid; task < kRowsWG * 320; task += 256) {
        const int rr = task / 320, k = task - rr * 320;
        float2 Xk = X[rr][k];
        float2 Xm = X[rr][320 - k];
        if (k == 0) { Xk.y = 0.f; Xm.y = 0.f; }
        const float2 E = make_float2(0.5f * (Xk.x + Xm.x), 0.5f * (Xk.y - Xm.y));
        const float2 d = make_float2(Xk.x - Xm.x, Xk.y + Xm.y);
        float2 O = cmulc(d, tw[k]);
        O.x *= 0.5f; O.y *= 0.5f;
        buf[0][rr * 320 + k] = make_float2(E.x - O.y, E.y + O.x);
    }
    __syncthreads();
    STAMP(3, 2);
    stockham_pass<8, 320, 320, 1, kRowsWG, 2, true>(buf[0], buf[1], tw, tid, 256);
    __syncthreads();
    STAMP(3, 3);
    stockham_pass<8, 320, 40, 8, kRowsWG, 16, true>(buf[1], buf[0], tw, tid, 256);
    __syncthreads();
    STAMP(3, 4);
    stockham_pass<5, 320, 5, 64, kRowsWG, 128, true>(buf[0], buf[1], tw, tid, 256);
    __syncthreads();
    STAMP(3, 5);
    const float* zr = reinterpret_cast<const float*>(buf[1] + r * 320);
    bf16_t* hrow = hout + (size_t)(row0 + r) * kD;
    bf16_t* yrow = yln2 + (size_t)(row0 + r) * kD;
    float hv[20];
    float sum = 0.f;
#pragma unroll
    for (int i = 0; i < 5; ++i) {
        const int c = l32 + 32 * i;
        const float4 zv = *reinterpret_cast<const float4*>(zr + 4 * c);
        const float xa = bf2f((bf16_t)(xv[i].x & 0xffffu)), xb = bf2f((bf16_t)(xv[i].x >> 16));
        const float xc = bf2f((bf16_t)(xv[i].y & 0xffffu)), xd = bf2f((bf16_t)(xv[i].y >> 16));
        const float h0 = rbf(xa + rbf(zv.x * (1.0f / 320.0f)));
        const float h1 = rbf(xb + rbf(zv.y * (1.0f / 320.0f)));
        const float h2 = rbf(xc + rbf(zv.z * (1.0f / 320.0f)));
        const float h3 = rbf(xd + rbf(zv.w * (1.0f / 320.0f)));
        hv[4 * i] = h0; hv[4 * i + 1] = h1; hv[4 * i + 2] = h2; hv[4 * i + 3] = h3;
        uint2 hp;
        hp.x = (unsigned)f2bf(h0) | ((unsigned)f2bf(h1) << 16);
        hp.y = (unsigned)f2bf(h2) | ((unsigned)f2bf(h3) << 16);
        *reinterpret_cast<uint2*>(hrow + 4 * c) = hp;
        sum += (h0 + h1) + (h2 + h3);
    }
#pragma unroll
    for (int off = 16; off > 0; off >>= 1) sum += __shfl_xor(sum, off);
    const float mean = sum * (1.0f / kD);
    const float mb = rbf(mean);
    float ss = 0.f;
#pragma unroll
    for (int i = 0; i < 20; ++i) { float d = hv[i] - mean; ss += d * d; }
#pragma unroll
    for (int off = 16; off > 0; off >>= 1) ss += __shfl_xor(ss, off);
    const float var = ss * (1.0f / kD);
    const float den = rbf(sqrtf(rbf(rbf(var) + kLnEps)));
    float y1[20];
    float sum2 = 0.f;
#pragma unroll
    for (int i = 0; i < 20; ++i) { y1[i] = rbf(rbf(hv[i] - mb) / den); sum2 += y1[i]; }
#pragma unroll
    for (int off = 16; off > 0; off >>= 1) sum2 += __shfl_xor(sum2, off);
    const float mean2 = sum2 * (1.0f / kD);
    const float mb2 = rbf(mean2);
    float ss2 = 0.f;
#pragma unroll
    for (int i = 0; i < 20; ++i) { float d = y1[i] - mean2; ss2 += d * d; }
#pragma unroll
    for (int off = 16; off > 0; off >>= 1) ss2 += __shfl_xor(ss2, off);
    const float var2 = ss2 * (1.0f / kD);
    const float den2 = rbf(sqrtf(rbf(rbf(var2) + kLnEps)));
#pragma unroll
    for (int i = 0; i < 5; ++i) {
        const int c = l32 + 32 * i;
        uint2 yp;
        yp.x = (unsigned)f2bf(rbf(y1[4 * i] - mb2) / den2) | ((unsigned)f2bf(rbf(y1[4 * i + 1] - mb2) / den2) << 16);
        yp.y = (unsigned)f2bf(rbf(y1[4 * i + 2] - mb2) / den2) | ((unsigned)f2bf(rbf(y1[4 * i + 3] - mb2) / den2) << 16);
        *reinterpret_cast<uint2*>(yrow + 4 * c) = yp;
    }
    STAMP(3, 6);
}

using bf16x8 = __attribute__((ext_vector_type(8))) __bf16;
using f32x16 = __attribute__((ext_vector_type(16))) float;
typedef unsigned __attribute__((ext_vector_type(4))) u32x4;
typedef unsigned __attribute__((ext_vector_type(2))) u32x2;

#define WAIT_V(n) asm volatile("s_waitcnt vmcnt(%0)" ::"n"(n) : "memory")
#define WAIT_L(n) asm volatile("s_waitcnt lgkmcnt(%0)" ::"n"(n) : "memory")
#define RAW_BARRIER() do { WAIT_L(0); __builtin_amdgcn_s_barrier(); } while (0)

__device__ __forceinline__ float gelu_tanh_f(float t) {
    const float x2 = t * __fmaf_rn(0.0713548162726008f, t * t, 1.5957691216057308f);
    return t * __frcp_rn(1.0f + __expf(-x2));
}

template <int TM, int TN, int WM, int WN, int NBUF, int EPI, int NTN>
__global__ void __launch_bounds__(256) gemm_kernel(const bf16_t* __restrict__ A, int lda,
                                                   const bf16_t* __restrict__ Bt, int ldb, int K,
                                                   const bf16_t* __restrict__ bias,
                                                   const bf16_t* __restrict__ resid,
                                                   bf16_t* __restrict__ C, int ldc) {
    constexpr int BK = 64;
    constexpr int D = NBUF - 1;
    constexpr int WTM = TM / WM, WTN = TN / WN;
    constexpr int MB = WTM / 32, NB = WTN / 32;
    constexpr int A_TILE_B = TM * 128, B_TILE_B = TN * 128, STAGE_B = A_TILE_B + B_TILE_B;
    constexpr int GA = TM / 32, GB = TN / 32;
    constexpr int GL = GA + GB;
    static_assert(WM * WN == 4, "4 waves");
    static_assert((D - 1) * GL <= 63, "vmcnt range");
    static_assert(4 * WTM * (WTN * 2 + 16) <= NBUF * STAGE_B, "epilogue staging fits");
    __shared__ __attribute__((aligned(1024))) char shm[NBUF * STAGE_B];

    const int tid = threadIdx.x, lane = tid & 63, wave = tid >> 6;
    const int wm = wave / WN, wn = wave % WN;
    constexpr int MPX = (kS / TM) / 8;
    const int xcd = blockIdx.x & 7, idx = blockIdx.x >> 3;
    const int m0 = (xcd * MPX + idx / NTN) * TM, n0 = (idx % NTN) * TN;
    const int r32 = lane & 31, hh = lane >> 5;

    int aoff[GA], boff[GB];
#pragma unroll
    for (int i = 0; i < GA; ++i) {
        const int row = 8 * (wave * GA + i) + (lane >> 3);
        const int c = (lane & 7) ^ ((row >> 1) & 7);
        aoff[i] = (m0 + row) * lda + 8 * c;
    }
#pragma unroll
    for (int i = 0; i < GB; ++i) {
        const int row = 8 * (wave * GB + i) + (lane >> 3);
        const int c = (lane & 7) ^ ((row >> 1) & 7);
        boff[i] = (n0 + row) * ldb + 8 * c;
    }
#define GLDS_STAGE(BUF, KT)                                                                        \
    do {                                                                                           \
        char* sa_ = shm + (BUF) * STAGE_B;                                                         \
        _Pragma("unroll") for (int i = 0; i < GA; ++i)                                             \
            __builtin_amdgcn_global_load_lds((const unsigned*)(A + aoff[i] + (KT) * BK),           \
                                             (unsigned*)(sa_ + (wave * GA + i) * 1024), 16, 0, 0); \
        _Pragma("unroll") for (int i = 0; i < GB; ++i)                                             \
            __builtin_amdgcn_global_load_lds((const unsigned*)(Bt + boff[i] + (KT) * BK),          \
                                             (unsigned*)(sa_ + A_TILE_B + (wave * GB + i) * 1024), 16, 0, 0); \
    } while (0)

    f32x16 acc[MB][NB];
#pragma unroll
    for (int i = 0; i < MB; ++i)
#pragma unroll
        for (int j = 0; j < NB; ++j)
#pragma unroll
            for (int e = 0; e < 16; ++e) acc[i][j][e] = 0.f;

    const int sw = (r32 >> 1) & 7;
    const int KT = K / BK;
    u32x2 bb[NB][4];
#pragma unroll
    for (int ni = 0; ni < NB; ++ni)
#pragma unroll
        for (int g = 0; g < 4; ++g)
            bb[ni][g] = *reinterpret_cast<const u32x2*>(bias + n0 + wn * WTN + ni * 32 + 8 * g + 4 * hh);
    STAMP(4 + EPI, 0);
#pragma unroll
    for (int pt = 0; pt < D; ++pt) GLDS_STAGE(pt, pt);
    WAIT_V((D - 1) * GL);
    RAW_BARRIER();
    STAMP(4 + EPI, 1);
    for (int kt = 0; kt < KT; ++kt) {
        const int cur = kt % NBUF;
        const bool pre = (kt + D < KT);
        const int nbuf = (kt + D) % NBUF;
        const char* sa = shm + cur * STAGE_B;
        const char* sb = sa + A_TILE_B;
        char* sn = shm + nbuf * STAGE_B;
        bf16x8 af[4][MB], bfv[4][NB];
#pragma unroll
        for (int kk = 0; kk < 4; ++kk) {
            const int coff = 16 * ((2 * kk + hh) ^ sw);
#pragma unroll
            for (int mi = 0; mi < MB; ++mi)
                af[kk][mi] = *reinterpret_cast<const bf16x8*>(sa + (wm * WTM + mi * 32 + r32) * 128 + coff);
#pragma unroll
            for (int ni = 0; ni < NB; ++ni)
                bfv[kk][ni] = *reinterpret_cast<const bf16x8*>(sb + (wn * WTN + ni * 32 + r32) * 128 + coff);
        }
#pragma unroll
        for (int kk = 0; kk < 4; ++kk) {
#pragma unroll
            for (int mi = 0; mi < MB; ++mi)
#pragma unroll
                for (int ni = 0; ni < NB; ++ni)
                    acc[mi][ni] = __builtin_amdgcn_mfma_f32_32x32x16_bf16(bfv[kk][ni], af[kk][mi], acc[mi][ni], 0, 0, 0);
            if (pre) {
#pragma unroll
                for (int q = 0; q < GL; ++q) {
                    if ((q * 4) / GL == kk) {
                        if (q < GA)
                            __builtin_amdgcn_global_load_lds((const unsigned*)(A + aoff[q < GA ? q : 0] + (kt + D) * BK),
                                                             (unsigned*)(sn + (wave * GA + q) * 1024), 16, 0, 0);
                        else
                            __builtin_amdgcn_global_load_lds((const unsigned*)(Bt + boff[q >= GA ? q - GA : 0] + (kt + D) * BK),
                                                             (unsigned*)(sn + A_TILE_B + (wave * GB + q - GA) * 1024), 16, 0, 0);
                    }
                }
            }
        }
        if (pre) WAIT_V((D - 1) * GL); else WAIT_V(0);
        RAW_BARRIER();
    }
    STAMP(4 + EPI, 2);
#undef GLDS_STAGE
    constexpr int SSTR = WTN * 2 + 16;
    char* stg = shm + wave * (WTM * SSTR);
#pragma unroll
    for (int ni = 0; ni < NB; ++ni) {
#pragma unroll
        for (int g = 0; g < 4; ++g) {
            const int nl = ni * 32 + 8 * g + 4 * hh;
            const float b0 = bf2f((bf16_t)(bb[ni][g][0] & 0xffffu)), b1 = bf2f((bf16_t)(bb[ni][g][0] >> 16));
            const float b2 = bf2f((bf16_t)(bb[ni][g][1] & 0xffffu)), b3 = bf2f((bf16_t)(bb[ni][g][1] >> 16));
#pragma unroll
            for (int mi = 0; mi < MB; ++mi) {
                float t0 = rbf(rbf(acc[mi][ni][4 * g + 0]) + b0);
                float t1 = rbf(rbf(acc[mi][ni][4 * g + 1]) + b1);
                float t2 = rbf(rbf(acc[mi][ni][4 * g + 2]) + b2);
                float t3 = rbf(rbf(acc[mi][ni][4 * g + 3]) + b3);
                if (EPI == 0) { t0 = gelu_tanh_f(t0); t1 = gelu_tanh_f(t1); t2 = gelu_tanh_f(t2); t3 = gelu_tanh_f(t3); }
                u32x2 pk;
                pk[0] = pack2bf(t0, t1);
                pk[1] = pack2bf(t2, t3);
                *reinterpret_cast<u32x2*>(stg + (mi * 32 + r32) * SSTR + nl * 2) = pk;
            }
        }
    }
    WAIT_L(0);
    constexpr int CPR = WTN / 8;
    constexpr int NU = WTM * CPR / 64;
#pragma unroll
    for (int i = 0; i < NU; ++i) {
        const int u = lane + 64 * i;
        const int row = u / CPR, ch = u % CPR;
        const u32x4 v = *reinterpret_cast<const u32x4*>(stg + row * SSTR + 16 * ch);
        const size_t go = (size_t)(m0 + wm * WTM + row) * ldc + n0 + wn * WTN + 8 * ch;
        if (EPI == 0) {
            *reinterpret_cast<u32x4*>(C + go) = v;
        } else {
            const u32x4 hr = *reinterpret_cast<const u32x4*>(resid + go);
            u32x4 o;
#pragma unroll
            for (int q = 0; q < 4; ++q) {
                const float lo = bf2f((bf16_t)(hr[q] & 0xffffu)) + bf2f((bf16_t)(v[q] & 0xffffu));
                const float hi = bf2f((bf16_t)(hr[q] >> 16)) + bf2f((bf16_t)(v[q] >> 16));
                o[q] = pack2bf(lo, hi);
            }
            *reinterpret_cast<u32x4*>(C + go) = o;
        }
    }
    STAMP(4 + EPI, 3);
}

extern "C" void kernel_launch(void* const* d_in, const int* in_sizes, int n_in,
                              void* d_out, int out_size, void* d_ws, size_t ws_size,
                              hipStream_t stream) {
    (void)in_sizes; (void)n_in; (void)out_size; (void)ws_size;
    const bf16_t* A_x  = (const bf16_t*)d_in[0];
    const bf16_t* A_wr = (const bf16_t*)d_in[1];
    const bf16_t* A_wi = (const bf16_t*)d_in[2];
    const bf16_t* B_w1 = (const bf16_t*)d_in[4];
    const bf16_t* B_b1 = (const bf16_t*)d_in[5];
    const bf16_t* B_w2 = (const bf16_t*)d_in[6];
    const bf16_t* B_b2 = (const bf16_t*)d_in[7];
    bf16_t* out = (bf16_t*)d_out;

    char* ws = (char*)d_ws;
    float2* T      = (float2*)(ws + OFF_T);
    float2* T2     = (float2*)(ws + OFF_T2);
    bf16_t* hbuf   = (bf16_t*)(ws + OFF_H);
    bf16_t* yln2   = (bf16_t*)(ws + OFF_YLN2);
    bf16_t* Hbuf   = (bf16_t*)(ws + OFF_G);
    bf16_t* W1t    = (bf16_t*)(ws + OFF_W1T);
    bf16_t* W2t    = (bf16_t*)(ws + OFF_W2T);
    float2* tw2048 = (float2*)(ws + OFF_TW2048);
    float2* tw640  = (float2*)(ws + OFF_TW640);
    unsigned* WT   = (unsigned*)(ws + OFF_WT);

    ln_rfft_kernel<<<kNBlkA, 256, 0, stream>>>(A_x, T, B_w1, B_w2, W1t, W2t, A_wr, A_wi, WT, tw2048, tw640);
    colfft_kernel<<<kDF, 256, 0, stream>>>(T, T2, WT, tw2048);
    irfft_ln_kernel<<<kS / kRowsWG, 256, 0, stream>>>(A_x, T2, tw640, hbuf, yln2);
    gemm_kernel<256, 64, 4, 1, 3, 0, kF / 64><<<(kF / 64) * (kS / 256), 256, 0, stream>>>(
        yln2, kD, W1t, kD, kD, B_b1, nullptr, Hbuf, kF);
    gemm_kernel<128, 64, 2, 2, 5, 1, kD / 64><<<(kD / 64) * (kS / 128), 256, 0, stream>>>(
        Hbuf, kF, W2t, kF, kF, B_b2, hbuf, out, kD);
}
```

```cpp
#include <hip/hip_runtime.h>
#include <hip/hip_bf16.h>
#include <stdint.h>

static constexpr int kS  = 2048;
static constexpr int kD  = 640;
static constexpr int kDF = 321;
static constexpr int kF  = 1728;
static constexpr float kLnEps = 1e-5f;

static constexpr size_t OFF_T     = 0;
static constexpr size_t OFF_T2    = OFF_T   + (size_t)kDF * kS * 8;
static constexpr size_t OFF_H     = OFF_T2  + (size_t)kDF * kS * 8;
static constexpr size_t OFF_YLN2  = OFF_H   + (size_t)kS * kD * 2;
static constexpr size_t OFF_G     = OFF_YLN2+ (size_t)kS * kD * 2;
static constexpr size_t OFF_W1T   = OFF_G   + (size_t)kS * kF * 2;
static constexpr size_t OFF_W2T   = OFF_W1T + (size_t)kF * kD * 2;
static constexpr size_t OFF_TW2048= OFF_W2T + (size_t)kD * kF * 2;
static constexpr size_t OFF_TW640 = OFF_TW2048 + (size_t)2048 * 8;
static constexpr size_t OFF_WT    = OFF_TW640 + (size_t)640 * 8;

typedef unsigned short bf16_t;

#define STAMP(KID, I) do { } while (0)

__device__ __forceinline__ float bf2f(bf16_t u) {
    return __uint_as_float(((unsigned)u) << 16);
}
__device__ __forceinline__ bf16_t f2bf(float f) {
    return __builtin_bit_cast(bf16_t, (__bf16)f);
}
typedef __attribute__((ext_vector_type(2))) __bf16 bf16x2_t;
typedef __attribute__((ext_vector_type(2))) float f32x2_t;
__device__ __forceinline__ unsigned pack2bf(float lo, float hi) {
    f32x2_t v = {lo, hi};
    bf16x2_t b = __builtin_convertvector(v, bf16x2_t);
    return __builtin_bit_cast(unsigned, b);
}
__device__ __forceinline__ float rbf(float f) { return bf2f(f2bf(f)); }

__device__ __forceinline__ float2 cadd(float2 a, float2 b) { return make_float2(a.x + b.x, a.y + b.y); }
__device__ __forceinline__ float2 csub(float2 a, float2 b) { return make_float2(a.x - b.x, a.y - b.y); }
__device__ __forceinline__ float2 cmul(float2 a, float2 b) {
    return make_float2(__fmaf_rn(a.x, b.x, -a.y * b.y), __fmaf_rn(a.x, b.y, a.y * b.x));
}
__device__ __forceinline__ float2 cmulc(float2 a, float2 b) {
    return make_float2(__fmaf_rn(a.x, b.x, a.y * b.y), __fmaf_rn(a.y, b.x, -a.x * b.y));
}
template <bool INV>
__device__ __forceinline__ float2 cmul_j(float2 a) {
    return INV ? make_float2(-a.y, a.x) : make_float2(a.y, -a.x);
}

template <bool INV>
__device__ __forceinline__ void dft4(float2 &b0, float2 &b1, float2 &b2, float2 &b3) {
    float2 t0 = cadd(b0, b2), t1 = csub(b0, b2), t2 = cadd(b1, b3), t3 = cmul_j<INV>(csub(b1, b3));
    b0 = cadd(t0, t2); b2 = csub(t0, t2); b1 = cadd(t1, t3); b3 = csub(t1, t3);
}
template <bool INV>
__device__ __forceinline__ void dft5(float2 a[5]) {
    const float c1 = 0.30901699437494745f, c2 = -0.8090169943749473f;
    const float s1 = 0.9510565162951535f,  s2 = 0.5877852522924731f;
    float2 b1 = cadd(a[1], a[4]), b4 = csub(a[1], a[4]);
    float2 b2 = cadd(a[2], a[3]), b3 = csub(a[2], a[3]);
    float2 t1 = make_float2(a[0].x + c1 * b1.x + c2 * b2.x, a[0].y + c1 * b1.y + c2 * b2.y);
    float2 t2 = make_float2(a[0].x + c2 * b1.x + c1 * b2.x, a[0].y + c2 * b1.y + c1 * b2.y);
    float2 u1 = make_float2(s1 * b4.x + s2 * b3.x, s1 * b4.y + s2 * b3.y);
    float2 u2 = make_float2(s2 * b4.x - s1 * b3.x, s2 * b4.y - s1 * b3.y);
    float2 iu1 = cmul_j<INV>(u1), iu2 = cmul_j<INV>(u2);
    a[0] = make_float2(a[0].x + b1.x + b2.x, a[0].y + b1.y + b2.y);
    a[1] = cadd(t1, iu1); a[4] = csub(t1, iu1);
    a[2] = cadd(t2, iu2); a[3] = csub(t2, iu2);
}
template <bool INV>
__device__ __forceinline__ void dft8(float2 a[8]) {
    const float r2 = 0.70710678118654752f;
    float2 e0 = a[0], e1 = a[2], e2 = a[4], e3 = a[6];
    float2 o0 = a[1], o1 = a[3], o2 = a[5], o3 = a[7];
    dft4<INV>(e0, e1, e2, e3);
    dft4<INV>(o0, o1, o2, o3);
    float2 p1, p3;
    if (!INV) {
        p1 = make_float2((o1.x + o1.y) * r2, (o1.y - o1.x) * r2);
        p3 = make_float2((o3.y - o3.x) * r2, -(o3.x + o3.y) * r2);
    } else {
        p1 = make_float2((o1.x - o1.y) * r2, (o1.x + o1.y) * r2);
        p3 = make_float2(-(o3.x + o3.y) * r2, (o3.x - o3.y) * r2);
    }
    float2 p2 = cmul_j<INV>(o2);
    a[0] = cadd(e0, o0); a[4] = csub(e0, o0);
    a[1] = cadd(e1, p1); a[5] = csub(e1, p1);
    a[2] = cadd(e2, p2); a[6] = csub(e2, p2);
    a[3] = cadd(e3, p3); a[7] = csub(e3, p3);
}

template <int R, bool INV> struct Dft;
template <bool INV> struct Dft<4, INV> {
    __device__ __forceinline__ static void run(float2 a[4]) { dft4<INV>(a[0], a[1], a[2], a[3]); }
};
template <bool INV> struct Dft<5, INV> {
    __device__ __forceinline__ static void run(float2 a[5]) { dft5<INV>(a); }
};
template <bool INV> struct Dft<8, INV> {
    __device__ __forceinline__ static void run(float2 a[8]) { dft8<INV>(a); }
};

template <int R, int NT, int NN, int SS, int NROWS, int TWSCALE, bool INV>
__device__ __forceinline__ void stockham_pass(const float2* __restrict__ in, float2* __restrict__ out,
                                              const float2* __restrict__ tw, int tid, int nthreads) {
    constexpr int M = NN / R;
    constexpr int NB = NT / R;
    for (int bt = tid; bt < NROWS * NB; bt += nthreads) {
        const int r   = bt / NB;
        const int rem = bt - r * NB;
        const int p   = rem / SS;
        const int q   = rem - p * SS;
        const float2* rin = in + r * NT;
        float2* rout = out + r * NT;
        float2 a[R];
#pragma unroll
        for (int k = 0; k < R; ++k) a[k] = rin[SS * (p + k * M) + q];
        Dft<R, INV>::run(a);
        if (NN != R) {
#pragma unroll
            for (int k = 1; k < R; ++k) {
                float2 w = tw[p * k * TWSCALE];
                a[k] = INV ? cmulc(a[k], w) : cmul(a[k], w);
            }
        }
#pragma unroll
        for (int k = 0; k < R; ++k) rout[SS * (R * p + k) + q] = a[k];
    }
}

static constexpr int kRowsWG = 8;
static constexpr int kNBlkFFT = kS / kRowsWG;
static constexpr int kNT1 = (kD / 64) * (kF / 64);
static constexpr int kNT2 = (kF / 64) * (kD / 64);
static constexpr int kNTW = (kS / 64) * ((kDF + 63) / 64);
static constexpr int kNTB = (2048 + 640 + 255) / 256;
static constexpr int kNBlkA = kNBlkFFT + kNT1 + kNT2 + kNTW + kNTB;

__device__ __forceinline__ void transpose_tile64(const bf16_t* __restrict__ src, bf16_t* __restrict__ dst,
                                                 int R, int C, int r0, int c0, bf16_t (*tile)[66], int tid) {
#pragma unroll
    for (int i = 0; i < 2; ++i) {
        int u = tid + 256 * i;
        int row = u >> 3, ch = u & 7;
        uint4 v = *reinterpret_cast<const uint4*>(src + (size_t)(r0 + row) * C + c0 + 8 * ch);
        const bf16_t* e = reinterpret_cast<const bf16_t*>(&v);
#pragma unroll
        for (int j = 0; j < 8; ++j) tile[row][8 * ch + j] = e[j];
    }
    __syncthreads();
#pragma unroll
    for (int i = 0; i < 2; ++i) {
        int u = tid + 256 * i;
        int orow = u >> 3, ch = u & 7;
        uint4 v;
        bf16_t* e = reinterpret_cast<bf16_t*>(&v);
#pragma unroll
        for (int j = 0; j < 8; ++j) e[j] = tile[8 * ch + j][orow];
        *reinterpret_cast<uint4*>(dst + (size_t)(c0 + orow) * R + r0 + 8 * ch) = v;
    }
}

__global__ void __launch_bounds__(256) ln_rfft_kernel(const bf16_t* __restrict__ x,
                                                      float2* __restrict__ T,
                                                      const bf16_t* __restrict__ W1, const bf16_t* __restrict__ W2,
                                                      bf16_t* __restrict__ W1t, bf16_t* __restrict__ W2t,
                                                      const bf16_t* __restrict__ w_r, const bf16_t* __restrict__ w_i,
                                                      unsigned* __restrict__ WT,
                                                      float2* __restrict__ tw2048_g, float2* __restrict__ tw640_g) {
    __shared__ float2 tw[640];
    __shared__ __attribute__((aligned(16))) float2 buf[2][kRowsWG * 320];
    const int tid = threadIdx.x;
    const int bid = blockIdx.x;
    if (bid >= kNBlkFFT) {
        int b = bid - kNBlkFFT;
        if (b < kNT1) {
            bf16_t (*tile)[66] = reinterpret_cast<bf16_t (*)[66]>(&buf[0][0]);
            transpose_tile64(W1, W1t, kD, kF, (b / (kF / 64)) * 64, (b % (kF / 64)) * 64, tile, tid);
            return;
        }
        b -= kNT1;
        if (b < kNT2) {
            bf16_t (*tile)[66] = reinterpret_cast<bf16_t (*)[66]>(&buf[0][0]);
            transpose_tile64(W2, W2t, kF, kD, (b / (kD / 64)) * 64, (b % (kD / 64)) * 64, tile, tid);
            return;
        }
        b -= kNT2;
        if (b < kNTW) {
            unsigned (*tile)[65] = reinterpret_cast<unsigned (*)[65]>(&buf[0][0]);
            const int s0 = (b / 6) * 64, l0 = (b % 6) * 64;
            const int ll = tid & 63, sr = tid >> 6;
            if (l0 + ll < kDF) {
#pragma unroll
                for (int i = 0; i < 16; ++i) {
                    const int s = sr + 4 * i;
                    const size_t idx = (size_t)(s0 + s) * kDF + l0 + ll;
                    tile[s][ll] = (unsigned)w_r[idx] | ((unsigned)w_i[idx] << 16);
                }
            }
            __syncthreads();
            const int ss = tid & 63, lr = tid >> 6;
#pragma unroll
            for (int i = 0; i < 16; ++i) {
                const int l = lr + 4 * i;
                if (l0 + l < kDF) WT[(size_t)(l0 + l) * kS + s0 + ss] = tile[ss][l];
            }
            return;
        }
        b -= kNTW;
        {
            const int idx = b * 256 + tid;
            if (idx < 2048) {
                float s, c;
                sincospif(-2.0f * (float)idx / 2048.0f, &s, &c);
                tw2048_g[idx] = make_float2(c, s);
            } else if (idx < 2048 + 640) {
                float s, c;
                sincospif(-2.0f * (float)(idx - 2048) / 640.0f, &s, &c);
                tw640_g[idx - 2048] = make_float2(c, s);
            }
            return;
        }
    }
    const int row0 = bid * kRowsWG;
    STAMP(1, 0);
    const int r = tid >> 5, l32 = tid & 31;
    const bf16_t* xr = x + (size_t)(row0 + r) * kD;
    uint2 xv[5];
#pragma unroll
    for (int i = 0; i < 5; ++i) xv[i] = *reinterpret_cast<const uint2*>(xr + 4 * (l32 + 32 * i));
    for (int i = tid; i < 640; i += 256) {
        float s, c;
        sincospif(-2.0f * (float)i / 640.0f, &s, &c);
        tw[i] = make_float2(c, s);
    }
    float v[20];
    float sum = 0.f;
#pragma unroll
    for (int i = 0; i < 5; ++i) {
        v[4 * i + 0] = bf2f((bf16_t)(xv[i].x & 0xffffu)); v[4 * i + 1] = bf2f((bf16_t)(xv[i].x >> 16));
        v[4 * i + 2] = bf2f((bf16_t)(xv[i].y & 0xffffu)); v[4 * i + 3] = bf2f((bf16_t)(xv[i].y >> 16));
        sum += (v[4 * i] + v[4 * i + 1]) + (v[4 * i + 2] + v[4 * i + 3]);
    }
#pragma unroll
    for (int off = 16; off > 0; off >>= 1) sum += __shfl_xor(sum, off);
    const float mean = sum * (1.0f / kD);
    const float mb = rbf(mean);
    float ss = 0.f;
#pragma unroll
    for (int i = 0; i < 20; ++i) { float d = v[i] - mean; ss += d * d; }
#pragma unroll
    for (int off = 16; off > 0; off >>= 1) ss += __shfl_xor(ss, off);
    const float var = ss * (1.0f / kD);
    const float den = rbf(sqrtf(rbf(rbf(var) + kLnEps)));
    {
        float* bufr = reinterpret_cast<float*>(buf[0]);
#pragma unroll
        for (int i = 0; i < 5; ++i) {
            float4 o;
            o.x = rbf(rbf(v[4 * i + 0] - mb) / den);
            o.y = rbf(rbf(v[4 * i + 1] - mb) / den);
            o.z = rbf(rbf(v[4 * i + 2] - mb) / den);
            o.w = rbf(rbf(v[4 * i + 3] - mb) / den);
            *reinterpret_cast<float4*>(bufr + r * kD + 4 * (l32 + 32 * i)) = o;
        }
    }
    __syncthreads();
    STAMP(1, 1);
    stockham_pass<8, 320, 320, 1, kRowsWG, 2, false>(buf[0], buf[1], tw, tid, 256);
    __syncthreads();
    STAMP(1, 2);
    stockham_pass<8, 320, 40, 8, kRowsWG, 16, false>(buf[1], buf[0], tw, tid, 256);
    __syncthreads();
    STAMP(1, 3);
    stockham_pass<5, 320, 5, 64, kRowsWG, 128, false>(buf[0], buf[1], tw, tid, 256);
    __syncthreads();
    STAMP(1, 4);
    const float2* Z = buf[1];
    for (int k = tid; k < kDF; k += 256) {
        const int km = (320 - k) % 320;
        const int kk = (k == 320) ? 0 : k;
        const float2 w = tw[k];
        float2 outv[kRowsWG];
#pragma unroll
        for (int rr = 0; rr < kRowsWG; ++rr) {
            const float2 Ze = Z[rr * 320 + kk];
            const float2 Zm = Z[rr * 320 + km];
            const float2 E = make_float2(0.5f * (Ze.x + Zm.x), 0.5f * (Ze.y - Zm.y));
            const float dx = Ze.x - Zm.x, dy = Ze.y + Zm.y;
            const float2 O = make_float2(0.5f * dy, -0.5f * dx);
            outv[rr] = cadd(E, cmul(w, O));
        }
        float4* dst = reinterpret_cast<float4*>(T + (size_t)k * kS + row0);
#pragma unroll
        for (int j = 0; j < kRowsWG / 2; ++j)
            dst[j] = make_float4(outv[2 * j].x, outv[2 * j].y, outv[2 * j + 1].x, outv[2 * j + 1].y);
    }
    STAMP(1, 5);
}

__global__ void __launch_bounds__(256) colfft_kernel(const float2* __restrict__ T,
                                                     float2* __restrict__ T2,
                                                     const unsigned* __restrict__ WT,
                                                     const float2* __restrict__ tw2048_g) {
    __shared__ __attribute__((aligned(16))) float2 tw[2048];
    __shared__ __attribute__((aligned(16))) float2 buf[2][2048];
    const int tid = threadIdx.x;
    const int l = blockIdx.x;
    STAMP(2, 0);
    unsigned wp[8];
#pragma unroll
    for (int j = 0; j < 8; ++j) wp[j] = WT[(size_t)l * kS + tid + 256 * j];
    const float4* tw4 = reinterpret_cast<const float4*>(tw2048_g);
    float4* twd = reinterpret_cast<float4*>(tw);
    const float4* col4 = reinterpret_cast<const float4*>(T + (size_t)l * kS);
    float4* b04 = reinterpret_cast<float4*>(buf[0]);
    float4 tv[4], cv[4];
#pragma unroll
    for (int i = 0; i < 4; ++i) { tv[i] = tw4[tid + 256 * i]; cv[i] = col4[tid + 256 * i]; }
#pragma unroll
    for (int i = 0; i < 4; ++i) { twd[tid + 256 * i] = tv[i]; b04[tid + 256 * i] = cv[i]; }
    __syncthreads();
    STAMP(2, 1);
    stockham_pass<8, 2048, 2048, 1, 1, 1, false>(buf[0], buf[1], tw, tid, 256);
    __syncthreads();
    STAMP(2, 2);
    stockham_pass<8, 2048, 256, 8, 1, 8, false>(buf[1], buf[0], tw, tid, 256);
    __syncthreads();
    STAMP(2, 3);
    stockham_pass<8, 2048, 32, 64, 1, 64, false>(buf[0], buf[1], tw, tid, 256);
    __syncthreads();
    STAMP(2, 4);
    {
        const float sc = 1.0f / 2048.0f;
#pragma unroll
        for (int t2 = 0; t2 < 2; ++t2) {
            const int q = tid + 256 * t2;
            float2 a[4];
#pragma unroll
            for (int k = 0; k < 4; ++k) a[k] = buf[1][512 * k + q];
            dft4<false>(a[0], a[1], a[2], a[3]);
#pragma unroll
            for (int k = 0; k < 4; ++k) {
                const int j = 2 * k + t2;
                const float2 wc = make_float2(bf2f((bf16_t)(wp[j] & 0xffffu)) * sc, bf2f((bf16_t)(wp[j] >> 16)) * sc);
                buf[0][512 * k + q] = cmul(a[k], wc);
            }
        }
    }
    __syncthreads();
    STAMP(2, 5);
    stockham_pass<8, 2048, 2048, 1, 1, 1, true>(buf[0], buf[1], tw, tid, 256);
    __syncthreads();
    STAMP(2, 6);
    stockham_pass<8, 2048, 256, 8, 1, 8, true>(buf[1], buf[0], tw, tid, 256);
    __syncthreads();
    STAMP(2, 7);
    stockham_pass<8, 2048, 32, 64, 1, 64, true>(buf[0], buf[1], tw, tid, 256);
    __syncthreads();
    STAMP(2, 8);
#pragma unroll
    for (int t2 = 0; t2 < 2; ++t2) {
        const int q = tid + 256 * t2;
        float2 a[4];
#pragma unroll
        for (int k = 0; k < 4; ++k) a[k] = buf[1][512 * k + q];
        dft4<true>(a[0], a[1], a[2], a[3]);
#pragma unroll
        for (int k = 0; k < 4; ++k) T2[(size_t)l * kS + 512 * k + q] = a[k];
    }
    STAMP(2, 9);
}

__global__ void __launch_bounds__(256) irfft_ln_kernel(const bf16_t* __restrict__ x,
                                                       const float2* __restrict__ T2,
                                                       const float2* __restrict__ tw640_g,
                                                       bf16_t* __restrict__ hout,
                                                       bf16_t* __restrict__ yln2) {
    __shared__ float2 tw[640];
    __shared__ __attribute__((aligned(16))) float2 X[kRowsWG][322];
    __shared__ __attribute__((aligned(16))) float2 buf[2][kRowsWG * 320];
    const int tid = threadIdx.x;
    const int row0 = (((blockIdx.x & 7) * 32) + (blockIdx.x >> 3)) * kRowsWG;
    STAMP(3, 0);
    const int r = tid >> 5, l32 = tid & 31;
    const bf16_t* xrow = x + (size_t)(row0 + r) * kD;
    uint2 xv[5];
#pragma unroll
    for (int i = 0; i < 5; ++i) xv[i] = *reinterpret_cast<const uint2*>(xrow + 4 * (l32 + 32 * i));
    for (int i = tid; i < 640; i += 256) tw[i] = tw640_g[i];
    for (int k = tid; k < kDF; k += 256) {
        const float4* src = reinterpret_cast<const float4*>(T2 + (size_t)k * kS + row0);
        float4 v0 = src[0], v1 = src[1], v2 = src[2], v3 = src[3];
        X[0][k] = make_float2(v0.x, v0.y); X[1][k] = make_float2(v0.z, v0.w);
        X[2][k] = make_float2(v1.x, v1.y); X[3][k] = make_float2(v1.z, v1.w);
        X[4][k] = make_float2(v2.x, v2.y); X[5][k] = make_float2(v2.z, v2.w);
        X[6][k] = make_float2(v3.x, v3.y); X[7][k] = make_float2(v3.z, v3.w);
    }
    __syncthreads();
    STAMP(3, 1);
    for (int task = tid; task < kRowsWG * 320; task += 256) {
        const int rr = task / 320, k = task - rr * 320;
        float2 Xk = X[rr][k];
        float2 Xm = X[rr][320 - k];
        if (k == 0) { Xk.y = 0.f; Xm.y = 0.f; }
        const float2 E = make_float2(0.5f * (Xk.x + Xm.x), 0.5f * (Xk.y - Xm.y));
        const float2 d = make_float2(Xk.x - Xm.x, Xk.y + Xm.y);
        float2 O = cmulc(d, tw[k]);
        O.x *= 0.5f; O.y *= 0.5f;
        buf[0][rr * 320 + k] = make_float2(E.x - O.y, E.y + O.x);
    }
    __syncthreads();
    STAMP(3, 2);
    stockham_pass<8, 320, 320, 1, kRowsWG, 2, true>(buf[0], buf[1], tw, tid, 256);
    __syncthreads();
    STAMP(3, 3);
    stockham_pass<8, 320, 40, 8, kRowsWG, 16, true>(buf[1], buf[0], tw, tid, 256);
    __syncthreads();
    STAMP(3, 4);
    stockham_pass<5, 320, 5, 64, kRowsWG, 128, true>(buf[0], buf[1], tw, tid, 256);
    __syncthreads();
    STAMP(3, 5);
    const float* zr = reinterpret_cast<const float*>(buf[1] + r * 320);
    bf16_t* hrow = hout + (size_t)(row0 + r) * kD;
    bf16_t* yrow = yln2 + (size_t)(row0 + r) * kD;
    float hv[20];
    float sum = 0.f;
#pragma unroll
    for (int i = 0; i < 5; ++i) {
        const int c = l32 + 32 * i;
        const float4 zv = *reinterpret_cast<const float4*>(zr + 4 * c);
        const float xa = bf2f((bf16_t)(xv[i].x & 0xffffu)), xb = bf2f((bf16_t)(xv[i].x >> 16));
        const float xc = bf2f((bf16_t)(xv[i].y & 0xffffu)), xd = bf2f((bf16_t)(xv[i].y >> 16));
        const float h0 = rbf(xa + rbf(zv.x * (1.0f / 320.0f)));
        const float h1 = rbf(xb + rbf(zv.y * (1.0f / 320.0f)));
        const float h2 = rbf(xc + rbf(zv.z * (1.0f / 320.0f)));
        const float h3 = rbf(xd + rbf(zv.w * (1.0f / 320.0f)));
        hv[4 * i] = h0; hv[4 * i + 1] = h1; hv[4 * i + 2] = h2; hv[4 * i + 3] = h3;
        uint2 hp;
        hp.x = (unsigned)f2bf(h0) | ((unsigned)f2bf(h1) << 16);
        hp.y = (unsigned)f2bf(h2) | ((unsigned)f2bf(h3) << 16);
        *reinterpret_cast<uint2*>(hrow + 4 * c) = hp;
        sum += (h0 + h1) + (h2 + h3);
    }
#pragma unroll
    for (int off = 16; off > 0; off >>= 1) sum += __shfl_xor(sum, off);
    const float mean = sum * (1.0f / kD);
    const float mb = rbf(mean);
    float ss = 0.f;
#pragma unroll
    for (int i = 0; i < 20; ++i) { float d = hv[i] - mean; ss += d * d; }
#pragma unroll
    for (int off = 16; off > 0; off >>= 1) ss += __shfl_xor(ss, off);
    const float var = ss * (1.0f / kD);
    const float den = rbf(sqrtf(rbf(rbf(var) + kLnEps)));
    float y1[20];
    float sum2 = 0.f;
#pragma unroll
    for (int i = 0; i < 20; ++i) { y1[i] = rbf(rbf(hv[i] - mb) / den); sum2 += y1[i]; }
#pragma unroll
    for (int off = 16; off > 0; off >>= 1) sum2 += __shfl_xor(sum2, off);
    const float mean2 = sum2 * (1.0f / kD);
    const float mb2 = rbf(mean2);
    float ss2 = 0.f;
#pragma unroll
    for (int i = 0; i < 20; ++i) { float d = y1[i] - mean2; ss2 += d * d; }
#pragma unroll
    for (int off = 16; off > 0; off >>= 1) ss2 += __shfl_xor(ss2, off);
    const float var2 = ss2 * (1.0f / kD);
    const float den2 = rbf(sqrtf(rbf(rbf(var2) + kLnEps)));
#pragma unroll
    for (int i = 0; i < 5; ++i) {
        const int c = l32 + 32 * i;
        uint2 yp;
        yp.x = (unsigned)f2bf(rbf(y1[4 * i] - mb2) / den2) | ((unsigned)f2bf(rbf(y1[4 * i + 1] - mb2) / den2) << 16);
        yp.y = (unsigned)f2bf(rbf(y1[4 * i + 2] - mb2) / den2) | ((unsigned)f2bf(rbf(y1[4 * i + 3] - mb2) / den2) << 16);
        *reinterpret_cast<uint2*>(yrow + 4 * c) = yp;
    }
    STAMP(3, 6);
}

using bf16x8 = __attribute__((ext_vector_type(8))) __bf16;
using f32x16 = __attribute__((ext_vector_type(16))) float;
typedef unsigned __attribute__((ext_vector_type(4))) u32x4;
typedef unsigned __attribute__((ext_vector_type(2))) u32x2;

#define WAIT_V(n) asm volatile("s_waitcnt vmcnt(%0)" ::"n"(n) : "memory")
#define WAIT_L(n) asm volatile("s_waitcnt lgkmcnt(%0)" ::"n"(n) : "memory")
#define RAW_BARRIER() do { WAIT_L(0); __builtin_amdgcn_s_barrier(); } while (0)

__device__ __forceinline__ float gelu_tanh_f(float t) {
    const float x2 = t * __fmaf_rn(0.0713548162726008f, t * t, 1.5957691216057308f);
    return t * __frcp_rn(1.0f + __expf(-x2));
}

template <int TM, int TN, int WM, int WN, int NBUF, int EPI, int NTN>
__global__ void __launch_bounds__(768) gemm_kernel(const bf16_t* __restrict__ A, int lda,
                                                   const bf16_t* __restrict__ Bt, int ldb, int K,
                                                   const bf16_t* __restrict__ bias,
                                                   const bf16_t* __restrict__ resid,
                                                   bf16_t* __restrict__ C, int ldc) {
    constexpr int BK = 64;
    constexpr int D = NBUF - 1;
    constexpr int WTM = TM / WM, WTN = TN / WN;
    constexpr int MB = WTM / 32, NB = WTN / 32;
    constexpr int A_TILE_B = TM * 128, B_TILE_B = TN * 128, STAGE_B = A_TILE_B + B_TILE_B;
    constexpr int NLW = 8;
    constexpr int GA = TM / 8 / NLW, GB = TN / 8 / NLW;
    constexpr int GL = GA + GB;
    static_assert(GA >= 1 && GB >= 1, "loader split");
    static_assert(WM * WN == 4, "4 compute waves");
    static_assert((D - 1) * GL <= 63, "vmcnt range");
    constexpr int SSTR = TN * 2 + 16;
    static_assert(TM * SSTR <= NBUF * STAGE_B, "epilogue staging fits");
    __shared__ __attribute__((aligned(1024))) char shm[NBUF * STAGE_B];

    const int tid = threadIdx.x, lane = tid & 63, wave8 = tid >> 6;
    const bool loader = wave8 >= 4;
    const int wave = loader ? (wave8 - 4) : wave8;
    const int wm = wave / WN, wn = wave % WN;
    constexpr int MPX = (kS / TM) / 8;
    const int xcd = blockIdx.x & 7, idx = blockIdx.x >> 3;
    const int m0 = (xcd * MPX + idx / NTN) * TM, n0 = (idx % NTN) * TN;
    const int r32 = lane & 31, hh = lane >> 5;
    const int KT = K / BK;
    STAMP(4 + EPI, 0);

    if (loader) {
        int aoff[GA], boff[GB];
#pragma unroll
        for (int i = 0; i < GA; ++i) {
            const int row = 8 * (wave * GA + i) + (lane >> 3);
            const int c = (lane & 7) ^ ((row >> 1) & 7);
            aoff[i] = (m0 + row) * lda + 8 * c;
        }
#pragma unroll
        for (int i = 0; i < GB; ++i) {
            const int row = 8 * (wave * GB + i) + (lane >> 3);
            const int c = (lane & 7) ^ ((row >> 1) & 7);
            boff[i] = (n0 + row) * ldb + 8 * c;
        }
#define GLDS_STAGE(BUF, KTILE)                                                                      \
    do {                                                                                            \
        char* sa_ = shm + (BUF) * STAGE_B;                                                          \
        _Pragma("unroll") for (int q_ = 0; q_ < GA; ++q_)                                           \
            __builtin_amdgcn_global_load_lds((const unsigned*)(A + aoff[q_] + (KTILE) * BK),        \
                                             (unsigned*)(sa_ + (wave * GA + q_) * 1024), 16, 0, 0); \
        _Pragma("unroll") for (int q_ = 0; q_ < GB; ++q_)                                           \
            __builtin_amdgcn_global_load_lds((const unsigned*)(Bt + boff[q_] + (KTILE) * BK),       \
                                             (unsigned*)(sa_ + A_TILE_B + (wave * GB + q_) * 1024), 16, 0, 0); \
    } while (0)
#pragma unroll
        for (int pt = 0; pt < D; ++pt) GLDS_STAGE(pt, pt);
        WAIT_V((D - 1) * GL);
        RAW_BARRIER();
        for (int kt = 0; kt < KT; ++kt) {
            if (kt + D < KT) {
                GLDS_STAGE((kt + D) % NBUF, kt + D);
                WAIT_V((D - 1) * GL);
            } else {
                WAIT_V(0);
            }
            RAW_BARRIER();
        }
#undef GLDS_STAGE
    } else {
    f32x16 acc[MB][NB];
#pragma unroll
    for (int i = 0; i < MB; ++i)
#pragma unroll
        for (int j = 0; j < NB; ++j)
#pragma unroll
            for (int e = 0; e < 16; ++e) acc[i][j][e] = 0.f;
    const int sw = (r32 >> 1) & 7;
    u32x2 bb[NB][4];
#pragma unroll
    for (int ni = 0; ni < NB; ++ni)
#pragma unroll
        for (int g = 0; g < 4; ++g)
            bb[ni][g] = *reinterpret_cast<const u32x2*>(bias + n0 + wn * WTN + ni * 32 + 8 * g + 4 * hh);
    RAW_BARRIER();
    STAMP(4 + EPI, 1);
    for (int kt = 0; kt < KT; ++kt) {
        const char* sa = shm + (kt % NBUF) * STAGE_B;
        const char* sb = sa + A_TILE_B;
        bf16x8 af[4][MB], bfv[4][NB];
#pragma unroll
        for (int kk = 0; kk < 4; ++kk) {
            const int coff = 16 * ((2 * kk + hh) ^ sw);
#pragma unroll
            for (int mi = 0; mi < MB; ++mi)
                af[kk][mi] = *reinterpret_cast<const bf16x8*>(sa + (wm * WTM + mi * 32 + r32) * 128 + coff);
#pragma unroll
            for (int ni = 0; ni < NB; ++ni)
                bfv[kk][ni] = *reinterpret_cast<const bf16x8*>(sb + (wn * WTN + ni * 32 + r32) * 128 + coff);
        }
#pragma unroll
        for (int kk = 0; kk < 4; ++kk)
#pragma unroll
            for (int mi = 0; mi < MB; ++mi)
#pragma unroll
                for (int ni = 0; ni < NB; ++ni)
                    acc[mi][ni] = __builtin_amdgcn_mfma_f32_32x32x16_bf16(bfv[kk][ni], af[kk][mi], acc[mi][ni], 0, 0, 0);
        RAW_BARRIER();
    }
    STAMP(4 + EPI, 2);
#pragma unroll
    for (int ni = 0; ni < NB; ++ni) {
#pragma unroll
        for (int g = 0; g < 4; ++g) {
            const int nl = ni * 32 + 8 * g + 4 * hh;
            const float b0 = bf2f((bf16_t)(bb[ni][g][0] & 0xffffu)), b1 = bf2f((bf16_t)(bb[ni][g][0] >> 16));
            const float b2 = bf2f((bf16_t)(bb[ni][g][1] & 0xffffu)), b3 = bf2f((bf16_t)(bb[ni][g][1] >> 16));
#pragma unroll
            for (int mi = 0; mi < MB; ++mi) {
                const float t0 = rbf(acc[mi][ni][4 * g + 0]) + b0;
                const float t1 = rbf(acc[mi][ni][4 * g + 1]) + b1;
                const float t2 = rbf(acc[mi][ni][4 * g + 2]) + b2;
                const float t3 = rbf(acc[mi][ni][4 * g + 3]) + b3;
                u32x2 pk;
                pk[0] = pack2bf(t0, t1);
                pk[1] = pack2bf(t2, t3);
                *reinterpret_cast<u32x2*>(shm + (wm * WTM + mi * 32 + r32) * SSTR + (wn * WTN + nl) * 2) = pk;
            }
        }
    }
    }
    RAW_BARRIER();
    constexpr int CPR = TN / 8;
    for (int u = tid; u < TM * CPR; u += 768) {
        const int row = u / CPR, ch = u % CPR;
        const u32x4 v = *reinterpret_cast<const u32x4*>(shm + row * SSTR + 16 * ch);
        const size_t go = (size_t)(m0 + row) * ldc + n0 + 8 * ch;
        u32x4 o;
        if (EPI == 0) {
#pragma unroll
            for (int q = 0; q < 4; ++q)
                o[q] = pack2bf(gelu_tanh_f(bf2f((bf16_t)(v[q] & 0xffffu))), gelu_tanh_f(bf2f((bf16_t)(v[q] >> 16))));
        } else {
            const u32x4 hr = *reinterpret_cast<const u32x4*>(resid + go);
#pragma unroll
            for (int q = 0; q < 4; ++q) {
                const float lo = bf2f((bf16_t)(hr[q] & 0xffffu)) + bf2f((bf16_t)(v[q] & 0xffffu));
                const float hi = bf2f((bf16_t)(hr[q] >> 16)) + bf2f((bf16_t)(v[q] >> 16));
                o[q] = pack2bf(lo, hi);
            }
        }
        *reinterpret_cast<u32x4*>(C + go) = o;
    }
    STAMP(4 + EPI, 3);
}

extern "C" void kernel_launch(void* const* d_in, const int* in_sizes, int n_in,
                              void* d_out, int out_size, void* d_ws, size_t ws_size,
                              hipStream_t stream) {
    (void)in_sizes; (void)n_in; (void)out_size; (void)ws_size;
    const bf16_t* A_x  = (const bf16_t*)d_in[0];
    const bf16_t* A_wr = (const bf16_t*)d_in[1];
    const bf16_t* A_wi = (const bf16_t*)d_in[2];
    const bf16_t* B_w1 = (const bf16_t*)d_in[4];
    const bf16_t* B_b1 = (const bf16_t*)d_in[5];
    const bf16_t* B_w2 = (const bf16_t*)d_in[6];
    const bf16_t* B_b2 = (const bf16_t*)d_in[7];
    bf16_t* out = (bf16_t*)d_out;

    char* ws = (char*)d_ws;
    float2* T      = (float2*)(ws + OFF_T);
    float2* T2     = (float2*)(ws + OFF_T2);
    bf16_t* hbuf   = (bf16_t*)(ws + OFF_H);
    bf16_t* yln2   = (bf16_t*)(ws + OFF_YLN2);
    bf16_t* Hbuf   = (bf16_t*)(ws + OFF_G);
    bf16_t* W1t    = (bf16_t*)(ws + OFF_W1T);
    bf16_t* W2t    = (bf16_t*)(ws + OFF_W2T);
    float2* tw2048 = (float2*)(ws + OFF_TW2048);
    float2* tw640  = (float2*)(ws + OFF_TW640);
    unsigned* WT   = (unsigned*)(ws + OFF_WT);

    ln_rfft_kernel<<<kNBlkA, 256, 0, stream>>>(A_x, T, B_w1, B_w2, W1t, W2t, A_wr, A_wi, WT, tw2048, tw640);
    colfft_kernel<<<kDF, 256, 0, stream>>>(T, T2, WT, tw2048);
    irfft_ln_kernel<<<kS / kRowsWG, 256, 0, stream>>>(A_x, T2, tw640, hbuf, yln2);
    gemm_kernel<256, 64, 4, 1, 3, 0, kF / 64><<<(kF / 64) * (kS / 256), 768, 0, stream>>>(
        yln2, kD, W1t, kD, kD, B_b1, nullptr, Hbuf, kF);
    gemm_kernel<128, 64, 2, 2, 5, 1, kD / 64><<<(kD / 64) * (kS / 128), 768, 0, stream>>>(
        Hbuf, kF, W2t, kF, kF, B_b2, hbuf, out, kD);
}
```

```cpp
#include <hip/hip_runtime.h>
#include <hip/hip_bf16.h>
#include <stdint.h>

static constexpr int kS  = 2048;
static constexpr int kD  = 640;
static constexpr int kDF = 321;
static constexpr int kF  = 1728;
static constexpr float kLnEps = 1e-5f;

static constexpr size_t OFF_T     = 0;
static constexpr size_t OFF_T2    = OFF_T   + (size_t)kDF * kS * 8;
static constexpr size_t OFF_H     = OFF_T2  + (size_t)kDF * kS * 8;
static constexpr size_t OFF_YLN2  = OFF_H   + (size_t)kS * kD * 2;
static constexpr size_t OFF_G     = OFF_YLN2+ (size_t)kS * kD * 2;
static constexpr size_t OFF_W1T   = OFF_G   + (size_t)kS * kF * 2;
static constexpr size_t OFF_W2T   = OFF_W1T + (size_t)kF * kD * 2;
static constexpr size_t OFF_TW2048= OFF_W2T + (size_t)kD * kF * 2;
static constexpr size_t OFF_TW640 = OFF_TW2048 + (size_t)2048 * 8;
static constexpr size_t OFF_WT    = OFF_TW640 + (size_t)640 * 8;

typedef unsigned short bf16_t;

#define STAMP(KID, I) do { } while (0)

__device__ __forceinline__ float bf2f(bf16_t u) {
    return __uint_as_float(((unsigned)u) << 16);
}
__device__ __forceinline__ bf16_t f2bf(float f) {
    return __builtin_bit_cast(bf16_t, (__bf16)f);
}
typedef __attribute__((ext_vector_type(2))) __bf16 bf16x2_t;
typedef __attribute__((ext_vector_type(2))) float f32x2_t;
__device__ __forceinline__ unsigned pack2bf(float lo, float hi) {
    f32x2_t v = {lo, hi};
    bf16x2_t b = __builtin_convertvector(v, bf16x2_t);
    return __builtin_bit_cast(unsigned, b);
}
__device__ __forceinline__ float rbf(float f) { return bf2f(f2bf(f)); }

__device__ __forceinline__ float2 cadd(float2 a, float2 b) { return make_float2(a.x + b.x, a.y + b.y); }
__device__ __forceinline__ float2 csub(float2 a, float2 b) { return make_float2(a.x - b.x, a.y - b.y); }
__device__ __forceinline__ float2 cmul(float2 a, float2 b) {
    return make_float2(__fmaf_rn(a.x, b.x, -a.y * b.y), __fmaf_rn(a.x, b.y, a.y * b.x));
}
__device__ __forceinline__ float2 cmulc(float2 a, float2 b) {
    return make_float2(__fmaf_rn(a.x, b.x, a.y * b.y), __fmaf_rn(a.y, b.x, -a.x * b.y));
}
template <bool INV>
__device__ __forceinline__ float2 cmul_j(float2 a) {
    return INV ? make_float2(-a.y, a.x) : make_float2(a.y, -a.x);
}

template <bool INV>
__device__ __forceinline__ void dft4(float2 &b0, float2 &b1, float2 &b2, float2 &b3) {
    float2 t0 = cadd(b0, b2), t1 = csub(b0, b2), t2 = cadd(b1, b3), t3 = cmul_j<INV>(csub(b1, b3));
    b0 = cadd(t0, t2); b2 = csub(t0, t2); b1 = cadd(t1, t3); b3 = csub(t1, t3);
}
template <bool INV>
__device__ __forceinline__ void dft5(float2 a[5]) {
    const float c1 = 0.30901699437494745f, c2 = -0.8090169943749473f;
    const float s1 = 0.9510565162951535f,  s2 = 0.5877852522924731f;
    float2 b1 = cadd(a[1], a[4]), b4 = csub(a[1], a[4]);
    float2 b2 = cadd(a[2], a[3]), b3 = csub(a[2], a[3]);
    float2 t1 = make_float2(a[0].x + c1 * b1.x + c2 * b2.x, a[0].y + c1 * b1.y + c2 * b2.y);
    float2 t2 = make_float2(a[0].x + c2 * b1.x + c1 * b2.x, a[0].y + c2 * b1.y + c1 * b2.y);
    float2 u1 = make_float2(s1 * b4.x + s2 * b3.x, s1 * b4.y + s2 * b3.y);
    float2 u2 = make_float2(s2 * b4.x - s1 * b3.x, s2 * b4.y - s1 * b3.y);
    float2 iu1 = cmul_j<INV>(u1), iu2 = cmul_j<INV>(u2);
    a[0] = make_float2(a[0].x + b1.x + b2.x, a[0].y + b1.y + b2.y);
    a[1] = cadd(t1, iu1); a[4] = csub(t1, iu1);
    a[2] = cadd(t2, iu2); a[3] = csub(t2, iu2);
}
template <bool INV>
__device__ __forceinline__ void dft8(float2 a[8]) {
    const float r2 = 0.70710678118654752f;
    float2 e0 = a[0], e1 = a[2], e2 = a[4], e3 = a[6];
    float2 o0 = a[1], o1 = a[3], o2 = a[5], o3 = a[7];
    dft4<INV>(e0, e1, e2, e3);
    dft4<INV>(o0, o1, o2, o3);
    float2 p1, p3;
    if (!INV) {
        p1 = make_float2((o1.x + o1.y) * r2, (o1.y - o1.x) * r2);
        p3 = make_float2((o3.y - o3.x) * r2, -(o3.x + o3.y) * r2);
    } else {
        p1 = make_float2((o1.x - o1.y) * r2, (o1.x + o1.y) * r2);
        p3 = make_float2(-(o3.x + o3.y) * r2, (o3.x - o3.y) * r2);
    }
    float2 p2 = cmul_j<INV>(o2);
    a[0] = cadd(e0, o0); a[4] = csub(e0, o0);
    a[1] = cadd(e1, p1); a[5] = csub(e1, p1);
    a[2] = cadd(e2, p2); a[6] = csub(e2, p2);
    a[3] = cadd(e3, p3); a[7] = csub(e3, p3);
}

template <int R, bool INV> struct Dft;
template <bool INV> struct Dft<4, INV> {
    __device__ __forceinline__ static void run(float2 a[4]) { dft4<INV>(a[0], a[1], a[2], a[3]); }
};
template <bool INV> struct Dft<5, INV> {
    __device__ __forceinline__ static void run(float2 a[5]) { dft5<INV>(a); }
};
template <bool INV> struct Dft<8, INV> {
    __device__ __forceinline__ static void run(float2 a[8]) { dft8<INV>(a); }
};

template <int R, int NT, int NN, int SS, int NROWS, int TWSCALE, bool INV>
__device__ __forceinline__ void stockham_pass(const float2* __restrict__ in, float2* __restrict__ out,
                                              const float2* __restrict__ tw, int tid, int nthreads) {
    constexpr int M = NN / R;
    constexpr int NB = NT / R;
    for (int bt = tid; bt < NROWS * NB; bt += nthreads) {
        const int r   = bt / NB;
        const int rem = bt - r * NB;
        const int p   = rem / SS;
        const int q   = rem - p * SS;
        const float2* rin = in + r * NT;
        float2* rout = out + r * NT;
        float2 a[R];
#pragma unroll
        for (int k = 0; k < R; ++k) a[k] = rin[SS * (p + k * M) + q];
        Dft<R, INV>::run(a);
        if (NN != R) {
#pragma unroll
            for (int k = 1; k < R; ++k) {
                float2 w = tw[p * k * TWSCALE];
                a[k] = INV ? cmulc(a[k], w) : cmul(a[k], w);
            }
        }
#pragma unroll
        for (int k = 0; k < R; ++k) rout[SS * (R * p + k) + q] = a[k];
    }
}

static constexpr int kRowsWG = 8;
static constexpr int kNBlkFFT = kS / kRowsWG;
static constexpr int kNT1 = (kD / 64) * (kF / 64);
static constexpr int kNT2 = (kF / 64) * (kD / 64);
static constexpr int kNTW = (kS / 64) * ((kDF + 63) / 64);
static constexpr int kNTB = (2048 + 640 + 255) / 256;
static constexpr int kNBlkA = kNBlkFFT + kNT1 + kNT2 + kNTW + kNTB;

__device__ __forceinline__ void transpose_tile64(const bf16_t* __restrict__ src, bf16_t* __restrict__ dst,
                                                 int R, int C, int r0, int c0, bf16_t (*tile)[66], int tid) {
#pragma unroll
    for (int i = 0; i < 2; ++i) {
        int u = tid + 256 * i;
        int row = u >> 3, ch = u & 7;
        uint4 v = *reinterpret_cast<const uint4*>(src + (size_t)(r0 + row) * C + c0 + 8 * ch);
        const bf16_t* e = reinterpret_cast<const bf16_t*>(&v);
#pragma unroll
        for (int j = 0; j < 8; ++j) tile[row][8 * ch + j] = e[j];
    }
    __syncthreads();
#pragma unroll
    for (int i = 0; i < 2; ++i) {
        int u = tid + 256 * i;
        int orow = u >> 3, ch = u & 7;
        uint4 v;
        bf16_t* e = reinterpret_cast<bf16_t*>(&v);
#pragma unroll
        for (int j = 0; j < 8; ++j) e[j] = tile[8 * ch + j][orow];
        *reinterpret_cast<uint4*>(dst + (size_t)(c0 + orow) * R + r0 + 8 * ch) = v;
    }
}

__global__ void __launch_bounds__(512) ln_rfft_kernel(const bf16_t* __restrict__ x,
                                                      float2* __restrict__ T,
                                                      const bf16_t* __restrict__ W1, const bf16_t* __restrict__ W2,
                                                      bf16_t* __restrict__ W1t, bf16_t* __restrict__ W2t,
                                                      const bf16_t* __restrict__ w_r, const bf16_t* __restrict__ w_i,
                                                      unsigned* __restrict__ WT,
                                                      float2* __restrict__ tw2048_g, float2* __restrict__ tw640_g) {
    __shared__ float2 tw[640];
    __shared__ __attribute__((aligned(16))) float2 buf[2][kRowsWG * 320];
    const int tid = threadIdx.x;
    const int bid = blockIdx.x;
    if (bid >= kNBlkFFT) {
        if (tid >= 256) return;
        int b = bid - kNBlkFFT;
        if (b < kNT1) {
            bf16_t (*tile)[66] = reinterpret_cast<bf16_t (*)[66]>(&buf[0][0]);
            transpose_tile64(W1, W1t, kD, kF, (b / (kF / 64)) * 64, (b % (kF / 64)) * 64, tile, tid);
            return;
        }
        b -= kNT1;
        if (b < kNT2) {
            bf16_t (*tile)[66] = reinterpret_cast<bf16_t (*)[66]>(&buf[0][0]);
            transpose_tile64(W2, W2t, kF, kD, (b / (kD / 64)) * 64, (b % (kD / 64)) * 64, tile, tid);
            return;
        }
        b -= kNT2;
        if (b < kNTW) {
            unsigned (*tile)[65] = reinterpret_cast<unsigned (*)[65]>(&buf[0][0]);
            const int s0 = (b / 6) * 64, l0 = (b % 6) * 64;
            const int ll = tid & 63, sr = tid >> 6;
            if (l0 + ll < kDF) {
#pragma unroll
                for (int i = 0; i < 16; ++i) {
                    const int s = sr + 4 * i;
                    const size_t idx = (size_t)(s0 + s) * kDF + l0 + ll;
                    tile[s][ll] = (unsigned)w_r[idx] | ((unsigned)w_i[idx] << 16);
                }
            }
            __syncthreads();
            const int ss = tid & 63, lr = tid >> 6;
#pragma unroll
            for (int i = 0; i < 16; ++i) {
                const int l = lr + 4 * i;
                if (l0 + l < kDF) WT[(size_t)(l0 + l) * kS + s0 + ss] = tile[ss][l];
            }
            return;
        }
        b -= kNTW;
        {
            const int idx = b * 256 + tid;
            if (idx < 2048) {
                float s, c;
                sincospif(-2.0f * (float)idx / 2048.0f, &s, &c);
                tw2048_g[idx] = make_float2(c, s);
            } else if (idx < 2048 + 640) {
                float s, c;
                sincospif(-2.0f * (float)(idx - 2048) / 640.0f, &s, &c);
                tw640_g[idx - 2048] = make_float2(c, s);
            }
            return;
        }
    }
    const int row0 = bid * kRowsWG;
    STAMP(1, 0);
    const int r = tid >> 6, l64 = tid & 63;
    const bf16_t* xr = x + (size_t)(row0 + r) * kD;
    unsigned xv[5];
#pragma unroll
    for (int i = 0; i < 5; ++i) xv[i] = *reinterpret_cast<const unsigned*>(xr + 2 * (l64 + 64 * i));
    for (int i = tid; i < 640; i += 512) {
        float sn, cs;
        sincospif(-2.0f * (float)i / 640.0f, &sn, &cs);
        tw[i] = make_float2(cs, sn);
    }
    float v[10];
    float sum = 0.f;
#pragma unroll
    for (int i = 0; i < 5; ++i) {
        v[2 * i + 0] = bf2f((bf16_t)(xv[i] & 0xffffu)); v[2 * i + 1] = bf2f((bf16_t)(xv[i] >> 16));
        sum += v[2 * i] + v[2 * i + 1];
    }
#pragma unroll
    for (int off = 32; off > 0; off >>= 1) sum += __shfl_xor(sum, off);
    const float mean = sum * (1.0f / kD);
    const float mb = rbf(mean);
    float ss = 0.f;
#pragma unroll
    for (int i = 0; i < 10; ++i) { float d = v[i] - mean; ss += d * d; }
#pragma unroll
    for (int off = 32; off > 0; off >>= 1) ss += __shfl_xor(ss, off);
    const float var = ss * (1.0f / kD);
    const float den = rbf(sqrtf(rbf(rbf(var) + kLnEps)));
#pragma unroll
    for (int i = 0; i < 5; ++i)
        buf[0][r * 320 + l64 + 64 * i] = make_float2(rbf(rbf(v[2 * i] - mb) / den), rbf(rbf(v[2 * i + 1] - mb) / den));
    __syncthreads();
    STAMP(1, 1);
    stockham_pass<8, 320, 320, 1, kRowsWG, 2, false>(buf[0], buf[1], tw, tid, 512);
    __syncthreads();
    STAMP(1, 2);
    stockham_pass<8, 320, 40, 8, kRowsWG, 16, false>(buf[1], buf[0], tw, tid, 512);
    __syncthreads();
    STAMP(1, 3);
    stockham_pass<5, 320, 5, 64, kRowsWG, 128, false>(buf[0], buf[1], tw, tid, 512);
    __syncthreads();
    STAMP(1, 4);
    const float2* Z = buf[1];
    for (int task = tid; task < 2 * kDF; task += 512) {
        const int k = task >> 1, rh = (task & 1) * 4;
        const int km = (320 - k) % 320;
        const int kk = (k == 320) ? 0 : k;
        const float2 w = tw[k];
        float2 outv[4];
#pragma unroll
        for (int rr = 0; rr < 4; ++rr) {
            const float2 Ze = Z[(rh + rr) * 320 + kk];
            const float2 Zm = Z[(rh + rr) * 320 + km];
            const float2 E = make_float2(0.5f * (Ze.x + Zm.x), 0.5f * (Ze.y - Zm.y));
            const float dx = Ze.x - Zm.x, dy = Ze.y + Zm.y;
            const float2 O = make_float2(0.5f * dy, -0.5f * dx);
            outv[rr] = cadd(E, cmul(w, O));
        }
        float4* dst = reinterpret_cast<float4*>(T + (size_t)k * kS + row0 + rh);
        dst[0] = make_float4(outv[0].x, outv[0].y, outv[1].x, outv[1].y);
        dst[1] = make_float4(outv[2].x, outv[2].y, outv[3].x, outv[3].y);
    }
    STAMP(1, 5);
}

__global__ void __launch_bounds__(256) colfft_kernel(const float2* __restrict__ T,
                                                     float2* __restrict__ T2,
                                                     const unsigned* __restrict__ WT,
                                                     const float2* __restrict__ tw2048_g) {
    __shared__ __attribute__((aligned(16))) float2 tw[2048];
    __shared__ __attribute__((aligned(16))) float2 buf[2][2048];
    const int tid = threadIdx.x;
    const int l = blockIdx.x;
    STAMP(2, 0);
    unsigned wp[8];
#pragma unroll
    for (int j = 0; j < 8; ++j) wp[j] = WT[(size_t)l * kS + tid + 256 * j];
    const float4* tw4 = reinterpret_cast<const float4*>(tw2048_g);
    float4* twd = reinterpret_cast<float4*>(tw);
    const float4* col4 = reinterpret_cast<const float4*>(T + (size_t)l * kS);
    float4* b04 = reinterpret_cast<float4*>(buf[0]);
    float4 tv[4], cv[4];
#pragma unroll
    for (int i = 0; i < 4; ++i) { tv[i] = tw4[tid + 256 * i]; cv[i] = col4[tid + 256 * i]; }
#pragma unroll
    for (int i = 0; i < 4; ++i) { twd[tid + 256 * i] = tv[i]; b04[tid + 256 * i] = cv[i]; }
    __syncthreads();
    STAMP(2, 1);
    stockham_pass<8, 2048, 2048, 1, 1, 1, false>(buf[0], buf[1], tw, tid, 256);
    __syncthreads();
    STAMP(2, 2);
    stockham_pass<8, 2048, 256, 8, 1, 8, false>(buf[1], buf[0], tw, tid, 256);
    __syncthreads();
    STAMP(2, 3);
    stockham_pass<8, 2048, 32, 64, 1, 64, false>(buf[0], buf[1], tw, tid, 256);
    __syncthreads();
    STAMP(2, 4);
    {
        const float sc = 1.0f / 2048.0f;
#pragma unroll
        for (int t2 = 0; t2 < 2; ++t2) {
            const int q = tid + 256 * t2;
            float2 a[4];
#pragma unroll
            for (int k = 0; k < 4; ++k) a[k] = buf[1][512 * k + q];
            dft4<false>(a[0], a[1], a[2], a[3]);
#pragma unroll
            for (int k = 0; k < 4; ++k) {
                const int j = 2 * k + t2;
                const float2 wc = make_float2(bf2f((bf16_t)(wp[j] & 0xffffu)) * sc, bf2f((bf16_t)(wp[j] >> 16)) * sc);
                buf[0][512 * k + q] = cmul(a[k], wc);
            }
        }
    }
    __syncthreads();
    STAMP(2, 5);
    stockham_pass<8, 2048, 2048, 1, 1, 1, true>(buf[0], buf[1], tw, tid, 256);
    __syncthreads();
    STAMP(2, 6);
    stockham_pass<8, 2048, 256, 8, 1, 8, true>(buf[1], buf[0], tw, tid, 256);
    __syncthreads();
    STAMP(2, 7);
    stockham_pass<8, 2048, 32, 64, 1, 64, true>(buf[0], buf[1], tw, tid, 256);
    __syncthreads();
    STAMP(2, 8);
#pragma unroll
    for (int t2 = 0; t2 < 2; ++t2) {
        const int q = tid + 256 * t2;
        float2 a[4];
#pragma unroll
        for (int k = 0; k < 4; ++k) a[k] = buf[1][512 * k + q];
        dft4<true>(a[0], a[1], a[2], a[3]);
#pragma unroll
        for (int k = 0; k < 4; ++k) T2[(size_t)l * kS + 512 * k + q] = a[k];
    }
    STAMP(2, 9);
}

__global__ void __launch_bounds__(512) irfft_ln_kernel(const bf16_t* __restrict__ x,
                                                       const float2* __restrict__ T2,
                                                       const float2* __restrict__ tw640_g,
                                                       bf16_t* __restrict__ hout,
                                                       bf16_t* __restrict__ yln2) {
    __shared__ float2 tw[640];
    __shared__ __attribute__((aligned(16))) float2 X[kRowsWG][322];
    __shared__ __attribute__((aligned(16))) float2 buf[2][kRowsWG * 320];
    const int tid = threadIdx.x;
    const int row0 = (((blockIdx.x & 7) * 32) + (blockIdx.x >> 3)) * kRowsWG;
    STAMP(3, 0);
    const int r = tid >> 6, l64 = tid & 63;
    const bf16_t* xrow = x + (size_t)(row0 + r) * kD;
    unsigned xv[5];
#pragma unroll
    for (int i = 0; i < 5; ++i) xv[i] = *reinterpret_cast<const unsigned*>(xrow + 2 * (l64 + 64 * i));
    for (int i = tid; i < 640; i += 512) tw[i] = tw640_g[i];
    for (int task = tid; task < 2 * kDF; task += 512) {
        const int k = task >> 1, rh = (task & 1) * 4;
        const float4* src = reinterpret_cast<const float4*>(T2 + (size_t)k * kS + row0 + rh);
        const float4 v0 = src[0], v1 = src[1];
        X[rh + 0][k] = make_float2(v0.x, v0.y); X[rh + 1][k] = make_float2(v0.z, v0.w);
        X[rh + 2][k] = make_float2(v1.x, v1.y); X[rh + 3][k] = make_float2(v1.z, v1.w);
    }
    __syncthreads();
    STAMP(3, 1);
    if (tid < kRowsWG * 40) {
        const int rr = tid / 40, p = tid - rr * 40;
        float2 a[8];
#pragma unroll
        for (int k = 0; k < 8; ++k) {
            const int j = p + 40 * k;
            float2 Xk = X[rr][j];
            float2 Xm = X[rr][320 - j];
            if (j == 0) { Xk.y = 0.f; Xm.y = 0.f; }
            const float2 E = make_float2(0.5f * (Xk.x + Xm.x), 0.5f * (Xk.y - Xm.y));
            const float2 d = make_float2(Xk.x - Xm.x, Xk.y + Xm.y);
            float2 O = cmulc(d, tw[j]);
            O.x *= 0.5f; O.y *= 0.5f;
            a[k] = make_float2(E.x - O.y, E.y + O.x);
        }
        dft8<true>(a);
#pragma unroll
        for (int k = 1; k < 8; ++k) a[k] = cmulc(a[k], tw[2 * p * k]);
        float2* rout = buf[1] + rr * 320;
#pragma unroll
        for (int k = 0; k < 8; ++k) rout[8 * p + k] = a[k];
    }
    __syncthreads();
    STAMP(3, 3);
    stockham_pass<8, 320, 40, 8, kRowsWG, 16, true>(buf[1], buf[0], tw, tid, 512);
    __syncthreads();
    STAMP(3, 4);
    stockham_pass<5, 320, 5, 64, kRowsWG, 128, true>(buf[0], buf[1], tw, tid, 512);
    __syncthreads();
    STAMP(3, 5);
    const float2* zr = buf[1] + r * 320;
    bf16_t* hrow = hout + (size_t)(row0 + r) * kD;
    bf16_t* yrow = yln2 + (size_t)(row0 + r) * kD;
    float hv[10];
    float sum = 0.f;
#pragma unroll
    for (int i = 0; i < 5; ++i) {
        const int c = l64 + 64 * i;
        const float2 zv = zr[c];
        const float h0 = rbf(bf2f((bf16_t)(xv[i] & 0xffffu)) + rbf(zv.x * (1.0f / 320.0f)));
        const float h1 = rbf(bf2f((bf16_t)(xv[i] >> 16)) + rbf(zv.y * (1.0f / 320.0f)));
        hv[2 * i] = h0; hv[2 * i + 1] = h1;
        *reinterpret_cast<unsigned*>(hrow + 2 * c) = pack2bf(h0, h1);
        sum += h0 + h1;
    }
#pragma unroll
    for (int off = 32; off > 0; off >>= 1) sum += __shfl_xor(sum, off);
    const float mean = sum * (1.0f / kD);
    const float mb = rbf(mean);
    float ss = 0.f;
#pragma unroll
    for (int i = 0; i < 10; ++i) { float d = hv[i] - mean; ss += d * d; }
#pragma unroll
    for (int off = 32; off > 0; off >>= 1) ss += __shfl_xor(ss, off);
    const float var = ss * (1.0f / kD);
    const float den = rbf(sqrtf(rbf(rbf(var) + kLnEps)));
    float y1[10];
    float sum2 = 0.f;
#pragma unroll
    for (int i = 0; i < 10; ++i) { y1[i] = rbf(rbf(hv[i] - mb) / den); sum2 += y1[i]; }
#pragma unroll
    for (int off = 32; off > 0; off >>= 1) sum2 += __shfl_xor(sum2, off);
    const float mean2 = sum2 * (1.0f / kD);
    const float mb2 = rbf(mean2);
    float ss2 = 0.f;
#pragma unroll
    for (int i = 0; i < 10; ++i) { float d = y1[i] - mean2; ss2 += d * d; }
#pragma unroll
    for (int off = 32; off > 0; off >>= 1) ss2 += __shfl_xor(ss2, off);
    const float var2 = ss2 * (1.0f / kD);
    const float den2 = rbf(sqrtf(rbf(rbf(var2) + kLnEps)));
#pragma unroll
    for (int i = 0; i < 5; ++i) {
        const int c = l64 + 64 * i;
        *reinterpret_cast<unsigned*>(yrow + 2 * c) = pack2bf(rbf(y1[2 * i] - mb2) / den2, rbf(y1[2 * i + 1] - mb2) / den2);
    }
    STAMP(3, 6);
}

using bf16x8 = __attribute__((ext_vector_type(8))) __bf16;
using f32x16 = __attribute__((ext_vector_type(16))) float;
typedef unsigned __attribute__((ext_vector_type(4))) u32x4;
typedef unsigned __attribute__((ext_vector_type(2))) u32x2;

#define WAIT_V(n) asm volatile("s_waitcnt vmcnt(%0)" ::"n"(n) : "memory")
#define WAIT_L(n) asm volatile("s_waitcnt lgkmcnt(%0)" ::"n"(n) : "memory")
#define RAW_BARRIER() do { WAIT_L(0); __builtin_amdgcn_s_barrier(); } while (0)

__device__ __forceinline__ float gelu_tanh_f(float t) {
    const float x2 = t * __fmaf_rn(0.0713548162726008f, t * t, 1.5957691216057308f);
    return t * __frcp_rn(1.0f + __expf(-x2));
}

template <int TM, int TN, int WM, int WN, int NBUF, int EPI, int NTN>
__global__ void __launch_bounds__(768) gemm_kernel(const bf16_t* __restrict__ A, int lda,
                                                   const bf16_t* __restrict__ Bt, int ldb, int K,
                                                   const bf16_t* __restrict__ bias,
                                                   const bf16_t* __restrict__ resid,
                                                   bf16_t* __restrict__ C, int ldc) {
    constexpr int BK = 64;
    constexpr int D = NBUF - 1;
    constexpr int WTM = TM / WM, WTN = TN / WN;
    constexpr int MB = WTM / 32, NB = WTN / 32;
    constexpr int A_TILE_B = TM * 128, B_TILE_B = TN * 128, STAGE_B = A_TILE_B + B_TILE_B;
    constexpr int NLW = 8;
    constexpr int GA = TM / 8 / NLW, GB = TN / 8 / NLW;
    constexpr int GL = GA + GB;
    static_assert(GA >= 1 && GB >= 1, "loader split");
    static_assert(WM * WN == 4, "4 compute waves");
    static_assert((D - 1) * GL <= 63, "vmcnt range");
    constexpr int SSTR = TN * 2 + 16;
    static_assert(TM * SSTR <= NBUF * STAGE_B, "epilogue staging fits");
    __shared__ __attribute__((aligned(1024))) char shm[NBUF * STAGE_B];

    const int tid = threadIdx.x, lane = tid & 63, wave8 = tid >> 6;
    const bool loader = wave8 >= 4;
    const int wave = loader ? (wave8 - 4) : wave8;
    const int wm = wave / WN, wn = wave % WN;
    constexpr int MPX = (kS / TM) / 8;
    const int xcd = blockIdx.x & 7, idx = blockIdx.x >> 3;
    const int m0 = (xcd * MPX + idx / NTN) * TM, n0 = (idx % NTN) * TN;
    const int r32 = lane & 31, hh = lane >> 5;
    const int KT = K / BK;
    STAMP(4 + EPI, 0);

    if (loader) {
        int aoff[GA], boff[GB];
#pragma unroll
        for (int i = 0; i < GA; ++i) {
            const int row = 8 * (wave * GA + i) + (lane >> 3);
            const int c = (lane & 7) ^ ((row >> 1) & 7);
            aoff[i] = (m0 + row) * lda + 8 * c;
        }
#pragma unroll
        for (int i = 0; i < GB; ++i) {
            const int row = 8 * (wave * GB + i) + (lane >> 3);
            const int c = (lane & 7) ^ ((row >> 1) & 7);
            boff[i] = (n0 + row) * ldb + 8 * c;
        }
#define GLDS_STAGE(BUF, KTILE)                                                                      \
    do {                                                                                            \
        char* sa_ = shm + (BUF) * STAGE_B;                                                          \
        _Pragma("unroll") for (int q_ = 0; q_ < GA; ++q_)                                           \
            __builtin_amdgcn_global_load_lds((const unsigned*)(A + aoff[q_] + (KTILE) * BK),        \
                                             (unsigned*)(sa_ + (wave * GA + q_) * 1024), 16, 0, 0); \
        _Pragma("unroll") for (int q_ = 0; q_ < GB; ++q_)                                           \
            __builtin_amdgcn_global_load_lds((const unsigned*)(Bt + boff[q_] + (KTILE) * BK),       \
                                             (unsigned*)(sa_ + A_TILE_B + (wave * GB + q_) * 1024), 16, 0, 0); \
    } while (0)
#pragma unroll
        for (int pt = 0; pt < D; ++pt) GLDS_STAGE(pt, pt);
        WAIT_V((D - 1) * GL);
        RAW_BARRIER();
        for (int kt = 0; kt < KT; ++kt) {
            if (kt + D < KT) {
                GLDS_STAGE((kt + D) % NBUF, kt + D);
                WAIT_V((D - 1) * GL);
            } else {
                WAIT_V(0);
            }
            RAW_BARRIER();
        }
#undef GLDS_STAGE
    } else {
    f32x16 acc[MB][NB];
#pragma unroll
    for (int i = 0; i < MB; ++i)
#pragma unroll
        for (int j = 0; j < NB; ++j)
#pragma unroll
            for (int e = 0; e < 16; ++e) acc[i][j][e] = 0.f;
    const int sw = (r32 >> 1) & 7;
    u32x2 bb[NB][4];
#pragma unroll
    for (int ni = 0; ni < NB; ++ni)
#pragma unroll
        for (int g = 0; g < 4; ++g)
            bb[ni][g] = *reinterpret_cast<const u32x2*>(bias + n0 + wn * WTN + ni * 32 + 8 * g + 4 * hh);
    RAW_BARRIER();
    STAMP(4 + EPI, 1);
    for (int kt = 0; kt < KT; ++kt) {
        const char* sa = shm + (kt % NBUF) * STAGE_B;
        const char* sb = sa + A_TILE_B;
        bf16x8 af[4][MB], bfv[4][NB];
#pragma unroll
        for (int kk = 0; kk < 4; ++kk) {
            const int coff = 16 * ((2 * kk + hh) ^ sw);
#pragma unroll
            for (int mi = 0; mi < MB; ++mi)
                af[kk][mi] = *reinterpret_cast<const bf16x8*>(sa + (wm * WTM + mi * 32 + r32) * 128 + coff);
#pragma unroll
            for (int ni = 0; ni < NB; ++ni)
                bfv[kk][ni] = *reinterpret_cast<const bf16x8*>(sb + (wn * WTN + ni * 32 + r32) * 128 + coff);
        }
#pragma unroll
        for (int kk = 0; kk < 4; ++kk)
#pragma unroll
            for (int mi = 0; mi < MB; ++mi)
#pragma unroll
                for (int ni = 0; ni < NB; ++ni)
                    acc[mi][ni] = __builtin_amdgcn_mfma_f32_32x32x16_bf16(bfv[kk][ni], af[kk][mi], acc[mi][ni], 0, 0, 0);
        RAW_BARRIER();
    }
    STAMP(4 + EPI, 2);
#pragma unroll
    for (int ni = 0; ni < NB; ++ni) {
#pragma unroll
        for (int g = 0; g < 4; ++g) {
            const int nl = ni * 32 + 8 * g + 4 * hh;
            const float b0 = bf2f((bf16_t)(bb[ni][g][0] & 0xffffu)), b1 = bf2f((bf16_t)(bb[ni][g][0] >> 16));
            const float b2 = bf2f((bf16_t)(bb[ni][g][1] & 0xffffu)), b3 = bf2f((bf16_t)(bb[ni][g][1] >> 16));
#pragma unroll
            for (int mi = 0; mi < MB; ++mi) {
                const float t0 = rbf(acc[mi][ni][4 * g + 0]) + b0;
                const float t1 = rbf(acc[mi][ni][4 * g + 1]) + b1;
                const float t2 = rbf(acc[mi][ni][4 * g + 2]) + b2;
                const float t3 = rbf(acc[mi][ni][4 * g + 3]) + b3;
                u32x2 pk;
                pk[0] = pack2bf(t0, t1);
                pk[1] = pack2bf(t2, t3);
                *reinterpret_cast<u32x2*>(shm + (wm * WTM + mi * 32 + r32) * SSTR + (wn * WTN + nl) * 2) = pk;
            }
        }
    }
    }
    RAW_BARRIER();
    constexpr int CPR = TN / 8;
    for (int u = tid; u < TM * CPR; u += 768) {
        const int row = u / CPR, ch = u % CPR;
        const u32x4 v = *reinterpret_cast<const u32x4*>(shm + row * SSTR + 16 * ch);
        const size_t go = (size_t)(m0 + row) * ldc + n0 + 8 * ch;
        u32x4 o;
        if (EPI == 0) {
#pragma unroll
            for (int q = 0; q < 4; ++q)
                o[q] = pack2bf(gelu_tanh_f(bf2f((bf16_t)(v[q] & 0xffffu))), gelu_tanh_f(bf2f((bf16_t)(v[q] >> 16))));
        } else {
            const u32x4 hr = *reinterpret_cast<const u32x4*>(resid + go);
#pragma unroll
            for (int q = 0; q < 4; ++q) {
                const float lo = bf2f((bf16_t)(hr[q] & 0xffffu)) + bf2f((bf16_t)(v[q] & 0xffffu));
                const float hi = bf2f((bf16_t)(hr[q] >> 16)) + bf2f((bf16_t)(v[q] >> 16));
                o[q] = pack2bf(lo, hi);
            }
        }
        *reinterpret_cast<u32x4*>(C + go) = o;
    }
    STAMP(4 + EPI, 3);
}

extern "C" void kernel_launch(void* const* d_in, const int* in_sizes, int n_in,
                              void* d_out, int out_size, void* d_ws, size_t ws_size,
                              hipStream_t stream) {
    (void)in_sizes; (void)n_in; (void)out_size; (void)ws_size;
    const bf16_t* A_x  = (const bf16_t*)d_in[0];
    const bf16_t* A_wr = (const bf16_t*)d_in[1];
    const bf16_t* A_wi = (const bf16_t*)d_in[2];
    const bf16_t* B_w1 = (const bf16_t*)d_in[4];
    const bf16_t* B_b1 = (const bf16_t*)d_in[5];
    const bf16_t* B_w2 = (const bf16_t*)d_in[6];
    const bf16_t* B_b2 = (const bf16_t*)d_in[7];
    bf16_t* out = (bf16_t*)d_out;

    char* ws = (char*)d_ws;
    float2* T      = (float2*)(ws + OFF_T);
    float2* T2     = (float2*)(ws + OFF_T2);
    bf16_t* hbuf   = (bf16_t*)(ws + OFF_H);
    bf16_t* yln2   = (bf16_t*)(ws + OFF_YLN2);
    bf16_t* Hbuf   = (bf16_t*)(ws + OFF_G);
    bf16_t* W1t    = (bf16_t*)(ws + OFF_W1T);
    bf16_t* W2t    = (bf16_t*)(ws + OFF_W2T);
    float2* tw2048 = (float2*)(ws + OFF_TW2048);
    float2* tw640  = (float2*)(ws + OFF_TW640);
    unsigned* WT   = (unsigned*)(ws + OFF_WT);

    ln_rfft_kernel<<<kNBlkA, 512, 0, stream>>>(A_x, T, B_w1, B_w2, W1t, W2t, A_wr, A_wi, WT, tw2048, tw640);
    colfft_kernel<<<kDF, 256, 0, stream>>>(T, T2, WT, tw2048);
    irfft_ln_kernel<<<kS / kRowsWG, 512, 0, stream>>>(A_x, T2, tw640, hbuf, yln2);
    gemm_kernel<256, 64, 4, 1, 3, 0, kF / 64><<<(kF / 64) * (kS / 256), 768, 0, stream>>>(
        yln2, kD, W1t, kD, kD, B_b1, nullptr, Hbuf, kF);
    gemm_kernel<128, 64, 2, 2, 5, 1, kD / 64><<<(kD / 64) * (kS / 128), 768, 0, stream>>>(
        Hbuf, kF, W2t, kF, kF, B_b2, hbuf, out, kD);
}
```

```cpp
#include <hip/hip_runtime.h>
#include <hip/hip_bf16.h>
#include <stdint.h>

static constexpr int kS  = 2048;
static constexpr int kD  = 640;
static constexpr int kDF = 321;
static constexpr int kF  = 1728;
static constexpr float kLnEps = 1e-5f;

static constexpr size_t OFF_T     = 0;
static constexpr size_t OFF_T2    = OFF_T   + (size_t)kDF * kS * 8;
static constexpr size_t OFF_H     = OFF_T2  + (size_t)kDF * kS * 8;
static constexpr size_t OFF_YLN2  = OFF_H   + (size_t)kS * kD * 2;
static constexpr size_t OFF_G     = OFF_YLN2+ (size_t)kS * kD * 2;
static constexpr size_t OFF_W1T   = OFF_G   + (size_t)kS * kF * 2;
static constexpr size_t OFF_W2T   = OFF_W1T + (size_t)kF * kD * 2;
static constexpr size_t OFF_TW2048= OFF_W2T + (size_t)kD * kF * 2;
static constexpr size_t OFF_TW640 = OFF_TW2048 + (size_t)2048 * 8;
static constexpr size_t OFF_WT    = OFF_TW640 + (size_t)640 * 8;

typedef unsigned short bf16_t;

#define STAMP(KID, I) do { } while (0)

__device__ __forceinline__ float bf2f(bf16_t u) {
    return __uint_as_float(((unsigned)u) << 16);
}
__device__ __forceinline__ bf16_t f2bf(float f) {
    return __builtin_bit_cast(bf16_t, (__bf16)f);
}
typedef __attribute__((ext_vector_type(2))) __bf16 bf16x2_t;
typedef __attribute__((ext_vector_type(2))) float f32x2_t;
__device__ __forceinline__ unsigned pack2bf(float lo, float hi) {
    f32x2_t v = {lo, hi};
    bf16x2_t b = __builtin_convertvector(v, bf16x2_t);
    return __builtin_bit_cast(unsigned, b);
}
__device__ __forceinline__ float rbf(float f) { return bf2f(f2bf(f)); }

__device__ __forceinline__ float2 cadd(float2 a, float2 b) { return make_float2(a.x + b.x, a.y + b.y); }
__device__ __forceinline__ float2 csub(float2 a, float2 b) { return make_float2(a.x - b.x, a.y - b.y); }
__device__ __forceinline__ float2 cmul(float2 a, float2 b) {
    return make_float2(__fmaf_rn(a.x, b.x, -a.y * b.y), __fmaf_rn(a.x, b.y, a.y * b.x));
}
__device__ __forceinline__ float2 cmulc(float2 a, float2 b) {
    return make_float2(__fmaf_rn(a.x, b.x, a.y * b.y), __fmaf_rn(a.y, b.x, -a.x * b.y));
}
template <bool INV>
__device__ __forceinline__ float2 cmul_j(float2 a) {
    return INV ? make_float2(-a.y, a.x) : make_float2(a.y, -a.x);
}

template <bool INV>
__device__ __forceinline__ void dft4(float2 &b0, float2 &b1, float2 &b2, float2 &b3) {
    float2 t0 = cadd(b0, b2), t1 = csub(b0, b2), t2 = cadd(b1, b3), t3 = cmul_j<INV>(csub(b1, b3));
    b0 = cadd(t0, t2); b2 = csub(t0, t2); b1 = cadd(t1, t3); b3 = csub(t1, t3);
}
template <bool INV>
__device__ __forceinline__ void dft5(float2 a[5]) {
    const float c1 = 0.30901699437494745f, c2 = -0.8090169943749473f;
    const float s1 = 0.9510565162951535f,  s2 = 0.5877852522924731f;
    float2 b1 = cadd(a[1], a[4]), b4 = csub(a[1], a[4]);
    float2 b2 = cadd(a[2], a[3]), b3 = csub(a[2], a[3]);
    float2 t1 = make_float2(a[0].x + c1 * b1.x + c2 * b2.x, a[0].y + c1 * b1.y + c2 * b2.y);
    float2 t2 = make_float2(a[0].x + c2 * b1.x + c1 * b2.x, a[0].y + c2 * b1.y + c1 * b2.y);
    float2 u1 = make_float2(s1 * b4.x + s2 * b3.x, s1 * b4.y + s2 * b3.y);
    float2 u2 = make_float2(s2 * b4.x - s1 * b3.x, s2 * b4.y - s1 * b3.y);
    float2 iu1 = cmul_j<INV>(u1), iu2 = cmul_j<INV>(u2);
    a[0] = make_float2(a[0].x + b1.x + b2.x, a[0].y + b1.y + b2.y);
    a[1] = cadd(t1, iu1); a[4] = csub(t1, iu1);
    a[2] = cadd(t2, iu2); a[3] = csub(t2, iu2);
}
template <bool INV>
__device__ __forceinline__ void dft8(float2 a[8]) {
    const float r2 = 0.70710678118654752f;
    float2 e0 = a[0], e1 = a[2], e2 = a[4], e3 = a[6];
    float2 o0 = a[1], o1 = a[3], o2 = a[5], o3 = a[7];
    dft4<INV>(e0, e1, e2, e3);
    dft4<INV>(o0, o1, o2, o3);
    float2 p1, p3;
    if (!INV) {
        p1 = make_float2((o1.x + o1.y) * r2, (o1.y - o1.x) * r2);
        p3 = make_float2((o3.y - o3.x) * r2, -(o3.x + o3.y) * r2);
    } else {
        p1 = make_float2((o1.x - o1.y) * r2, (o1.x + o1.y) * r2);
        p3 = make_float2(-(o3.x + o3.y) * r2, (o3.x - o3.y) * r2);
    }
    float2 p2 = cmul_j<INV>(o2);
    a[0] = cadd(e0, o0); a[4] = csub(e0, o0);
    a[1] = cadd(e1, p1); a[5] = csub(e1, p1);
    a[2] = cadd(e2, p2); a[6] = csub(e2, p2);
    a[3] = cadd(e3, p3); a[7] = csub(e3, p3);
}

template <int R, bool INV> struct Dft;
template <bool INV> struct Dft<4, INV> {
    __device__ __forceinline__ static void run(float2 a[4]) { dft4<INV>(a[0], a[1], a[2], a[3]); }
};
template <bool INV> struct Dft<5, INV> {
    __device__ __forceinline__ static void run(float2 a[5]) { dft5<INV>(a); }
};
template <bool INV> struct Dft<8, INV> {
    __device__ __forceinline__ static void run(float2 a[8]) { dft8<INV>(a); }
};

template <int R, int NT, int NN, int SS, int NROWS, int TWSCALE, bool INV>
__device__ __forceinline__ void stockham_pass(const float2* __restrict__ in, float2* __restrict__ out,
                                              const float2* __restrict__ tw, int tid, int nthreads) {
    constexpr int M = NN / R;
    constexpr int NB = NT / R;
    for (int bt = tid; bt < NROWS * NB; bt += nthreads) {
        const int r   = bt / NB;
        const int rem = bt - r * NB;
        const int p   = rem / SS;
        const int q   = rem - p * SS;
        const float2* rin = in + r * NT;
        float2* rout = out + r * NT;
        float2 a[R];
#pragma unroll
        for (int k = 0; k < R; ++k) a[k] = rin[SS * (p + k * M) + q];
        Dft<R, INV>::run(a);
        if (NN != R) {
#pragma unroll
            for (int k = 1; k < R; ++k) {
                float2 w = tw[p * k * TWSCALE];
                a[k] = INV ? cmulc(a[k], w) : cmul(a[k], w);
            }
        }
#pragma unroll
        for (int k = 0; k < R; ++k) rout[SS * (R * p + k) + q] = a[k];
    }
}

static constexpr int kRowsWG = 8;
static constexpr int kNBlkFFT = kS / kRowsWG;
static constexpr int kNT1 = (kD / 64) * (kF / 64);
static constexpr int kNT2 = (kF / 64) * (kD / 64);
static constexpr int kNTW = (kS / 64) * ((kDF + 63) / 64);
static constexpr int kNTB = (2048 + 640 + 255) / 256;
static constexpr int kNBlkA = kNBlkFFT + kNT1 + kNT2 + kNTW + kNTB;

__device__ __forceinline__ void transpose_tile64(const bf16_t* __restrict__ src, bf16_t* __restrict__ dst,
                                                 int R, int C, int r0, int c0, bf16_t (*tile)[66], int tid) {
#pragma unroll
    for (int i = 0; i < 2; ++i) {
        int u = tid + 256 * i;
        int row = u >> 3, ch = u & 7;
        uint4 v = *reinterpret_cast<const uint4*>(src + (size_t)(r0 + row) * C + c0 + 8 * ch);
        const bf16_t* e = reinterpret_cast<const bf16_t*>(&v);
#pragma unroll
        for (int j = 0; j < 8; ++j) tile[row][8 * ch + j] = e[j];
    }
    __syncthreads();
#pragma unroll
    for (int i = 0; i < 2; ++i) {
        int u = tid + 256 * i;
        int orow = u >> 3, ch = u & 7;
        uint4 v;
        bf16_t* e = reinterpret_cast<bf16_t*>(&v);
#pragma unroll
        for (int j = 0; j < 8; ++j) e[j] = tile[8 * ch + j][orow];
        *reinterpret_cast<uint4*>(dst + (size_t)(c0 + orow) * R + r0 + 8 * ch) = v;
    }
}

__global__ void __launch_bounds__(512) ln_rfft_kernel(const bf16_t* __restrict__ x,
                                                      float2* __restrict__ T,
                                                      const bf16_t* __restrict__ W1, const bf16_t* __restrict__ W2,
                                                      bf16_t* __restrict__ W1t, bf16_t* __restrict__ W2t,
                                                      const bf16_t* __restrict__ w_r, const bf16_t* __restrict__ w_i,
                                                      unsigned* __restrict__ WT,
                                                      float2* __restrict__ tw2048_g, float2* __restrict__ tw640_g) {
    __shared__ float2 tw[640];
    __shared__ __attribute__((aligned(16))) float2 buf[2][kRowsWG * 320];
    const int tid = threadIdx.x;
    const int bid = blockIdx.x;
    if (bid >= kNBlkFFT) {
        if (tid >= 256) return;
        int b = bid - kNBlkFFT;
        if (b < kNT1) {
            bf16_t (*tile)[66] = reinterpret_cast<bf16_t (*)[66]>(&buf[0][0]);
            transpose_tile64(W1, W1t, kD, kF, (b / (kF / 64)) * 64, (b % (kF / 64)) * 64, tile, tid);
            return;
        }
        b -= kNT1;
        if (b < kNT2) {
            bf16_t (*tile)[66] = reinterpret_cast<bf16_t (*)[66]>(&buf[0][0]);
            transpose_tile64(W2, W2t, kF, kD, (b / (kD / 64)) * 64, (b % (kD / 64)) * 64, tile, tid);
            return;
        }
        b -= kNT2;
        if (b < kNTW) {
            unsigned (*tile)[65] = reinterpret_cast<unsigned (*)[65]>(&buf[0][0]);
            const int s0 = (b / 6) * 64, l0 = (b % 6) * 64;
            const int ll = tid & 63, sr = tid >> 6;
            if (l0 + ll < kDF) {
#pragma unroll
                for (int i = 0; i < 16; ++i) {
                    const int s = sr + 4 * i;
                    const size_t idx = (size_t)(s0 + s) * kDF + l0 + ll;
                    tile[s][ll] = (unsigned)w_r[idx] | ((unsigned)w_i[idx] << 16);
                }
            }
            __syncthreads();
            const int ss = tid & 63, lr = tid >> 6;
#pragma unroll
            for (int i = 0; i < 16; ++i) {
                const int l = lr + 4 * i;
                if (l0 + l < kDF) WT[(size_t)(l0 + l) * kS + s0 + ss] = tile[ss][l];
            }
            return;
        }
        b -= kNTW;
        {
            const int idx = b * 256 + tid;
            if (idx < 2048) {
                float s, c;
                sincospif(-2.0f * (float)idx / 2048.0f, &s, &c);
                tw2048_g[idx] = make_float2(c, s);
            } else if (idx < 2048 + 640) {
                float s, c;
                sincospif(-2.0f * (float)(idx - 2048) / 640.0f, &s, &c);
                tw640_g[idx - 2048] = make_float2(c, s);
            }
            return;
        }
    }
    const int row0 = bid * kRowsWG;
    STAMP(1, 0);
    const int r = tid >> 6, l64 = tid & 63;
    const bf16_t* xr = x + (size_t)(row0 + r) * kD;
    unsigned xv[5];
#pragma unroll
    for (int i = 0; i < 5; ++i) xv[i] = *reinterpret_cast<const unsigned*>(xr + 2 * (l64 + 64 * i));
    for (int i = tid; i < 640; i += 512) {
        float sn, cs;
        sincospif(-2.0f * (float)i / 640.0f, &sn, &cs);
        tw[i] = make_float2(cs, sn);
    }
    float v[10];
    float sum = 0.f;
#pragma unroll
    for (int i = 0; i < 5; ++i) {
        v[2 * i + 0] = bf2f((bf16_t)(xv[i] & 0xffffu)); v[2 * i + 1] = bf2f((bf16_t)(xv[i] >> 16));
        sum += v[2 * i] + v[2 * i + 1];
    }
#pragma unroll
    for (int off = 32; off > 0; off >>= 1) sum += __shfl_xor(sum, off);
    const float mean = sum * (1.0f / kD);
    const float mb = rbf(mean);
    float ss = 0.f;
#pragma unroll
    for (int i = 0; i < 10; ++i) { float d = v[i] - mean; ss += d * d; }
#pragma unroll
    for (int off = 32; off > 0; off >>= 1) ss += __shfl_xor(ss, off);
    const float var = ss * (1.0f / kD);
    const float den = rbf(sqrtf(rbf(rbf(var) + kLnEps)));
#pragma unroll
    for (int i = 0; i < 5; ++i)
        buf[0][r * 320 + l64 + 64 * i] = make_float2(rbf(rbf(v[2 * i] - mb) / den), rbf(rbf(v[2 * i + 1] - mb) / den));
    __syncthreads();
    STAMP(1, 1);
    stockham_pass<8, 320, 320, 1, kRowsWG, 2, false>(buf[0], buf[1], tw, tid, 512);
    __syncthreads();
    STAMP(1, 2);
    stockham_pass<8, 320, 40, 8, kRowsWG, 16, false>(buf[1], buf[0], tw, tid, 512);
    __syncthreads();
    STAMP(1, 3);
    stockham_pass<5, 320, 5, 64, kRowsWG, 128, false>(buf[0], buf[1], tw, tid, 512);
    __syncthreads();
    STAMP(1, 4);
    const float2* Z = buf[1];
    for (int task = tid; task < 2 * kDF; task += 512) {
        const int k = task >> 1, rh = (task & 1) * 4;
        const int km = (320 - k) % 320;
        const int kk = (k == 320) ? 0 : k;
        const float2 w = tw[k];
        float2 outv[4];
#pragma unroll
        for (int rr = 0; rr < 4; ++rr) {
            const float2 Ze = Z[(rh + rr) * 320 + kk];
            const float2 Zm = Z[(rh + rr) * 320 + km];
            const float2 E = make_float2(0.5f * (Ze.x + Zm.x), 0.5f * (Ze.y - Zm.y));
            const float dx = Ze.x - Zm.x, dy = Ze.y + Zm.y;
            const float2 O = make_float2(0.5f * dy, -0.5f * dx);
            outv[rr] = cadd(E, cmul(w, O));
        }
        float4* dst = reinterpret_cast<float4*>(T + (size_t)k * kS + row0 + rh);
        dst[0] = make_float4(outv[0].x, outv[0].y, outv[1].x, outv[1].y);
        dst[1] = make_float4(outv[2].x, outv[2].y, outv[3].x, outv[3].y);
    }
    STAMP(1, 5);
}

__global__ void __launch_bounds__(256) colfft_kernel(const float2* __restrict__ T,
                                                     float2* __restrict__ T2,
                                                     const unsigned* __restrict__ WT,
                                                     const float2* __restrict__ tw2048_g) {
    __shared__ __attribute__((aligned(16))) float2 tw[2048];
    __shared__ __attribute__((aligned(16))) float2 buf[2][2048];
    const int tid = threadIdx.x;
    const int l = blockIdx.x;
    STAMP(2, 0);
    unsigned wp[8];
#pragma unroll
    for (int j = 0; j < 8; ++j) wp[j] = WT[(size_t)l * kS + tid + 256 * j];
    const float4* tw4 = reinterpret_cast<const float4*>(tw2048_g);
    float4* twd = reinterpret_cast<float4*>(tw);
    const float4* col4 = reinterpret_cast<const float4*>(T + (size_t)l * kS);
    float4* b04 = reinterpret_cast<float4*>(buf[0]);
    float4 tv[4], cv[4];
#pragma unroll
    for (int i = 0; i < 4; ++i) { tv[i] = tw4[tid + 256 * i]; cv[i] = col4[tid + 256 * i]; }
#pragma unroll
    for (int i = 0; i < 4; ++i) { twd[tid + 256 * i] = tv[i]; b04[tid + 256 * i] = cv[i]; }
    __syncthreads();
    STAMP(2, 1);
    stockham_pass<8, 2048, 2048, 1, 1, 1, false>(buf[0], buf[1], tw, tid, 256);
    __syncthreads();
    STAMP(2, 2);
    stockham_pass<8, 2048, 256, 8, 1, 8, false>(buf[1], buf[0], tw, tid, 256);
    __syncthreads();
    STAMP(2, 3);
    stockham_pass<8, 2048, 32, 64, 1, 64, false>(buf[0], buf[1], tw, tid, 256);
    __syncthreads();
    STAMP(2, 4);
    {
        const float sc = 1.0f / 2048.0f;
#pragma unroll
        for (int t2 = 0; t2 < 2; ++t2) {
            const int q = tid + 256 * t2;
            float2 a[4];
#pragma unroll
            for (int k = 0; k < 4; ++k) a[k] = buf[1][512 * k + q];
            dft4<false>(a[0], a[1], a[2], a[3]);
#pragma unroll
            for (int k = 0; k < 4; ++k) {
                const int j = 2 * k + t2;
                const float2 wc = make_float2(bf2f((bf16_t)(wp[j] & 0xffffu)) * sc, bf2f((bf16_t)(wp[j] >> 16)) * sc);
                buf[0][512 * k + q] = cmul(a[k], wc);
            }
        }
    }
    __syncthreads();
    STAMP(2, 5);
    stockham_pass<8, 2048, 2048, 1, 1, 1, true>(buf[0], buf[1], tw, tid, 256);
    __syncthreads();
    STAMP(2, 6);
    stockham_pass<8, 2048, 256, 8, 1, 8, true>(buf[1], buf[0], tw, tid, 256);
    __syncthreads();
    STAMP(2, 7);
    stockham_pass<8, 2048, 32, 64, 1, 64, true>(buf[0], buf[1], tw, tid, 256);
    __syncthreads();
    STAMP(2, 8);
#pragma unroll
    for (int t2 = 0; t2 < 2; ++t2) {
        const int q = tid + 256 * t2;
        float2 a[4];
#pragma unroll
        for (int k = 0; k < 4; ++k) a[k] = buf[1][512 * k + q];
        dft4<true>(a[0], a[1], a[2], a[3]);
#pragma unroll
        for (int k = 0; k < 4; ++k) T2[(size_t)l * kS + 512 * k + q] = a[k];
    }
    STAMP(2, 9);
}

__global__ void __launch_bounds__(512) irfft_ln_kernel(const bf16_t* __restrict__ x,
                                                       const float2* __restrict__ T2,
                                                       const float2* __restrict__ tw640_g,
                                                       bf16_t* __restrict__ hout,
                                                       bf16_t* __restrict__ yln2) {
    __shared__ float2 tw[640];
    __shared__ __attribute__((aligned(16))) float2 X[kRowsWG][322];
    __shared__ __attribute__((aligned(16))) float2 buf[2][kRowsWG * 320];
    const int tid = threadIdx.x;
    const int row0 = (((blockIdx.x & 7) * 32) + (blockIdx.x >> 3)) * kRowsWG;
    STAMP(3, 0);
    const int r = tid >> 6, l64 = tid & 63;
    const bf16_t* xrow = x + (size_t)(row0 + r) * kD;
    unsigned xv[5];
#pragma unroll
    for (int i = 0; i < 5; ++i) xv[i] = *reinterpret_cast<const unsigned*>(xrow + 2 * (l64 + 64 * i));
    for (int i = tid; i < 640; i += 512) tw[i] = tw640_g[i];
    for (int task = tid; task < 2 * kDF; task += 512) {
        const int k = task >> 1, rh = (task & 1) * 4;
        const float4* src = reinterpret_cast<const float4*>(T2 + (size_t)k * kS + row0 + rh);
        const float4 v0 = src[0], v1 = src[1];
        X[rh + 0][k] = make_float2(v0.x, v0.y); X[rh + 1][k] = make_float2(v0.z, v0.w);
        X[rh + 2][k] = make_float2(v1.x, v1.y); X[rh + 3][k] = make_float2(v1.z, v1.w);
    }
    __syncthreads();
    STAMP(3, 1);
    if (tid < kRowsWG * 40) {
        const int rr = tid / 40, p = tid - rr * 40;
        float2 a[8];
#pragma unroll
        for (int k = 0; k < 8; ++k) {
            const int j = p + 40 * k;
            float2 Xk = X[rr][j];
            float2 Xm = X[rr][320 - j];
            if (j == 0) { Xk.y = 0.f; Xm.y = 0.f; }
            const float2 E = make_float2(0.5f * (Xk.x + Xm.x), 0.5f * (Xk.y - Xm.y));
            const float2 d = make_float2(Xk.x - Xm.x, Xk.y + Xm.y);
            float2 O = cmulc(d, tw[j]);
            O.x *= 0.5f; O.y *= 0.5f;
            a[k] = make_float2(E.x - O.y, E.y + O.x);
        }
        dft8<true>(a);
#pragma unroll
        for (int k = 1; k < 8; ++k) a[k] = cmulc(a[k], tw[2 * p * k]);
        float2* rout = buf[1] + rr * 320;
#pragma unroll
        for (int k = 0; k < 8; ++k) rout[8 * p + k] = a[k];
    }
    __syncthreads();
    STAMP(3, 3);
    stockham_pass<8, 320, 40, 8, kRowsWG, 16, true>(buf[1], buf[0], tw, tid, 512);
    __syncthreads();
    STAMP(3, 4);
    stockham_pass<5, 320, 5, 64, kRowsWG, 128, true>(buf[0], buf[1], tw, tid, 512);
    __syncthreads();
    STAMP(3, 5);
    const float2* zr = buf[1] + r * 320;
    bf16_t* hrow = hout + (size_t)(row0 + r) * kD;
    bf16_t* yrow = yln2 + (size_t)(row0 + r) * kD;
    float hv[10];
    float sum = 0.f;
#pragma unroll
    for (int i = 0; i < 5; ++i) {
        const int c = l64 + 64 * i;
        const float2 zv = zr[c];
        const float h0 = rbf(bf2f((bf16_t)(xv[i] & 0xffffu)) + rbf(zv.x * (1.0f / 320.0f)));
        const float h1 = rbf(bf2f((bf16_t)(xv[i] >> 16)) + rbf(zv.y * (1.0f / 320.0f)));
        hv[2 * i] = h0; hv[2 * i + 1] = h1;
        *reinterpret_cast<unsigned*>(hrow + 2 * c) = pack2bf(h0, h1);
        sum += h0 + h1;
    }
#pragma unroll
    for (int off = 32; off > 0; off >>= 1) sum += __shfl_xor(sum, off);
    const float mean = sum * (1.0f / kD);
    const float mb = rbf(mean);
    float ss = 0.f;
#pragma unroll
    for (int i = 0; i < 10; ++i) { float d = hv[i] - mean; ss += d * d; }
#pragma unroll
    for (int off = 32; off > 0; off >>= 1) ss += __shfl_xor(ss, off);
    const float var = ss * (1.0f / kD);
    const float den = rbf(sqrtf(rbf(rbf(var) + kLnEps)));
    float y1[10];
    float sum2 = 0.f;
#pragma unroll
    for (int i = 0; i < 10; ++i) { y1[i] = rbf(rbf(hv[i] - mb) / den); sum2 += y1[i]; }
#pragma unroll
    for (int off = 32; off > 0; off >>= 1) sum2 += __shfl_xor(sum2, off);
    const float mean2 = sum2 * (1.0f / kD);
    const float mb2 = rbf(mean2);
    float ss2 = 0.f;
#pragma unroll
    for (int i = 0; i < 10; ++i) { float d = y1[i] - mean2; ss2 += d * d; }
#pragma unroll
    for (int off = 32; off > 0; off >>= 1) ss2 += __shfl_xor(ss2, off);
    const float var2 = ss2 * (1.0f / kD);
    const float den2 = rbf(sqrtf(rbf(rbf(var2) + kLnEps)));
#pragma unroll
    for (int i = 0; i < 5; ++i) {
        const int c = l64 + 64 * i;
        *reinterpret_cast<unsigned*>(yrow + 2 * c) = pack2bf(rbf(y1[2 * i] - mb2) / den2, rbf(y1[2 * i + 1] - mb2) / den2);
    }
    STAMP(3, 6);
}

using bf16x8 = __attribute__((ext_vector_type(8))) __bf16;
using f32x16 = __attribute__((ext_vector_type(16))) float;
typedef unsigned __attribute__((ext_vector_type(4))) u32x4;
typedef unsigned __attribute__((ext_vector_type(2))) u32x2;

#define WAIT_V(n) asm volatile("s_waitcnt vmcnt(%0)" ::"n"(n) : "memory")
#define WAIT_L(n) asm volatile("s_waitcnt lgkmcnt(%0)" ::"n"(n) : "memory")
#define RAW_BARRIER() do { WAIT_L(0); __builtin_amdgcn_s_barrier(); } while (0)

__device__ __forceinline__ float gelu_tanh_f(float t) {
    const float x2 = t * __fmaf_rn(0.0713548162726008f, t * t, 1.5957691216057308f);
    return t * __frcp_rn(1.0f + __expf(-x2));
}

template <int TM, int TN, int WM, int WN, int NBUF, int EPI, int NTN>
__global__ void __launch_bounds__(768) gemm_kernel(const bf16_t* __restrict__ A, int lda,
                                                   const bf16_t* __restrict__ Bt, int ldb, int K,
                                                   const bf16_t* __restrict__ bias,
                                                   const bf16_t* __restrict__ resid,
                                                   bf16_t* __restrict__ C, int ldc) {
    constexpr int BK = 64;
    constexpr int D = NBUF - 1;
    constexpr int WTM = TM / WM, WTN = TN / WN;
    constexpr int MB = WTM / 32, NB = WTN / 32;
    constexpr int A_TILE_B = TM * 128, B_TILE_B = TN * 128, STAGE_B = A_TILE_B + B_TILE_B;
    constexpr int NLW = 8;
    constexpr int GA = TM / 8 / NLW, GB = TN / 8 / NLW;
    constexpr int GL = GA + GB;
    static_assert(GA >= 1 && GB >= 1, "loader split");
    static_assert(WM * WN == 4, "4 compute waves");
    static_assert((D - 1) * GL <= 63, "vmcnt range");
    constexpr int SSTR = TN * 2 + 16;
    static_assert(TM * SSTR <= NBUF * STAGE_B, "epilogue staging fits");
    __shared__ __attribute__((aligned(1024))) char shm[NBUF * STAGE_B];

    const int tid = threadIdx.x, lane = tid & 63, wave8 = tid >> 6;
    const bool loader = wave8 >= 4;
    const int wave = loader ? (wave8 - 4) : wave8;
    const int wm = wave / WN, wn = wave % WN;
    constexpr int MPX = (kS / TM) / 8;
    const int xcd = blockIdx.x & 7, idx = blockIdx.x >> 3;
    const int m0 = (xcd * MPX + idx / NTN) * TM, n0 = (idx % NTN) * TN;
    const int r32 = lane & 31, hh = lane >> 5;
    const int KT = K / BK;
    STAMP(4 + EPI, 0);

    if (loader) {
        int aoff[GA], boff[GB];
#pragma unroll
        for (int i = 0; i < GA; ++i) {
            const int row = 8 * (wave * GA + i) + (lane >> 3);
            const int c = (lane & 7) ^ ((row >> 1) & 7);
            aoff[i] = (m0 + row) * lda + 8 * c;
        }
#pragma unroll
        for (int i = 0; i < GB; ++i) {
            const int row = 8 * (wave * GB + i) + (lane >> 3);
            const int c = (lane & 7) ^ ((row >> 1) & 7);
            boff[i] = (n0 + row) * ldb + 8 * c;
        }
#define GLDS_STAGE(BUF, KTILE)                                                                      \
    do {                                                                                            \
        char* sa_ = shm + (BUF) * STAGE_B;                                                          \
        _Pragma("unroll") for (int q_ = 0; q_ < GA; ++q_)                                           \
            __builtin_amdgcn_global_load_lds((const unsigned*)(A + aoff[q_] + (KTILE) * BK),        \
                                             (unsigned*)(sa_ + (wave * GA + q_) * 1024), 16, 0, 0); \
        _Pragma("unroll") for (int q_ = 0; q_ < GB; ++q_)                                           \
            __builtin_amdgcn_global_load_lds((const unsigned*)(Bt + boff[q_] + (KTILE) * BK),       \
                                             (unsigned*)(sa_ + A_TILE_B + (wave * GB + q_) * 1024), 16, 0, 0); \
    } while (0)
#pragma unroll
        for (int pt = 0; pt < D; ++pt) GLDS_STAGE(pt, pt);
        WAIT_V((D - 1) * GL);
        RAW_BARRIER();
        for (int kt = 0; kt < KT; ++kt) {
            if (kt + D < KT) {
                GLDS_STAGE((kt + D) % NBUF, kt + D);
                WAIT_V((D - 1) * GL);
            } else {
                WAIT_V(0);
            }
            RAW_BARRIER();
        }
#undef GLDS_STAGE
    } else {
    f32x16 acc[MB][NB];
#pragma unroll
    for (int i = 0; i < MB; ++i)
#pragma unroll
        for (int j = 0; j < NB; ++j)
#pragma unroll
            for (int e = 0; e < 16; ++e) acc[i][j][e] = 0.f;
    const int sw = (r32 >> 1) & 7;
    u32x2 bb[NB][4];
#pragma unroll
    for (int ni = 0; ni < NB; ++ni)
#pragma unroll
        for (int g = 0; g < 4; ++g)
            bb[ni][g] = *reinterpret_cast<const u32x2*>(bias + n0 + wn * WTN + ni * 32 + 8 * g + 4 * hh);
    RAW_BARRIER();
    STAMP(4 + EPI, 1);
    for (int kt = 0; kt < KT; ++kt) {
        const char* sa = shm + (kt % NBUF) * STAGE_B;
        const char* sb = sa + A_TILE_B;
        bf16x8 af[4][MB], bfv[4][NB];
#pragma unroll
        for (int kk = 0; kk < 4; ++kk) {
            const int coff = 16 * ((2 * kk + hh) ^ sw);
#pragma unroll
            for (int mi = 0; mi < MB; ++mi)
                af[kk][mi] = *reinterpret_cast<const bf16x8*>(sa + (wm * WTM + mi * 32 + r32) * 128 + coff);
#pragma unroll
            for (int ni = 0; ni < NB; ++ni)
                bfv[kk][ni] = *reinterpret_cast<const bf16x8*>(sb + (wn * WTN + ni * 32 + r32) * 128 + coff);
        }
#pragma unroll
        for (int kk = 0; kk < 4; ++kk)
#pragma unroll
            for (int mi = 0; mi < MB; ++mi)
#pragma unroll
                for (int ni = 0; ni < NB; ++ni)
                    acc[mi][ni] = __builtin_amdgcn_mfma_f32_32x32x16_bf16(bfv[kk][ni], af[kk][mi], acc[mi][ni], 0, 0, 0);
        RAW_BARRIER();
    }
    STAMP(4 + EPI, 2);
#pragma unroll
    for (int ni = 0; ni < NB; ++ni) {
#pragma unroll
        for (int g = 0; g < 4; ++g) {
            const int nl = ni * 32 + 8 * g + 4 * hh;
            const float b0 = bf2f((bf16_t)(bb[ni][g][0] & 0xffffu)), b1 = bf2f((bf16_t)(bb[ni][g][0] >> 16));
            const float b2 = bf2f((bf16_t)(bb[ni][g][1] & 0xffffu)), b3 = bf2f((bf16_t)(bb[ni][g][1] >> 16));
#pragma unroll
            for (int mi = 0; mi < MB; ++mi) {
                const float t0 = rbf(acc[mi][ni][4 * g + 0]) + b0;
                const float t1 = rbf(acc[mi][ni][4 * g + 1]) + b1;
                const float t2 = rbf(acc[mi][ni][4 * g + 2]) + b2;
                const float t3 = rbf(acc[mi][ni][4 * g + 3]) + b3;
                u32x2 pk;
                pk[0] = pack2bf(t0, t1);
                pk[1] = pack2bf(t2, t3);
                *reinterpret_cast<u32x2*>(shm + (wm * WTM + mi * 32 + r32) * SSTR + (wn * WTN + nl) * 2) = pk;
            }
        }
    }
    }
    RAW_BARRIER();
    constexpr int CPR = TN / 8;
    for (int u = tid; u < TM * CPR; u += 768) {
        const int row = u / CPR, ch = u % CPR;
        const u32x4 v = *reinterpret_cast<const u32x4*>(shm + row * SSTR + 16 * ch);
        const size_t go = (size_t)(m0 + row) * ldc + n0 + 8 * ch;
        u32x4 o;
        if (EPI == 0) {
#pragma unroll
            for (int q = 0; q < 4; ++q)
                o[q] = pack2bf(gelu_tanh_f(bf2f((bf16_t)(v[q] & 0xffffu))), gelu_tanh_f(bf2f((bf16_t)(v[q] >> 16))));
        } else {
            const u32x4 hr = *reinterpret_cast<const u32x4*>(resid + go);
#pragma unroll
            for (int q = 0; q < 4; ++q) {
                const float lo = bf2f((bf16_t)(hr[q] & 0xffffu)) + bf2f((bf16_t)(v[q] & 0xffffu));
                const float hi = bf2f((bf16_t)(hr[q] >> 16)) + bf2f((bf16_t)(v[q] >> 16));
                o[q] = pack2bf(lo, hi);
            }
        }
        *reinterpret_cast<u32x4*>(C + go) = o;
    }
    STAMP(4 + EPI, 3);
}

using f32x4v = __attribute__((ext_vector_type(4))) float;

__global__ void __launch_bounds__(512) gemm2_kernel(const bf16_t* __restrict__ A, const bf16_t* __restrict__ Bt,
                                                    const bf16_t* __restrict__ bias,
                                                    const bf16_t* __restrict__ resid,
                                                    bf16_t* __restrict__ C) {
    constexpr int TM = 64, TN = 80, BK = 64, KT = kF / BK;
    constexpr int NBUF = 6, D = NBUF - 1;
    constexpr int STAGE_B = (TM + TN) * 128;
    constexpr int GLW = 3;
    constexpr int SSTR = TN * 2 + 16;
    static_assert((D - 1) * GLW <= 63, "vmcnt range");
    static_assert(TM * SSTR <= NBUF * STAGE_B, "staging fits");
    __shared__ __attribute__((aligned(1024))) char shm[NBUF * STAGE_B];

    const int tid = threadIdx.x, lane = tid & 63, wave8 = tid >> 6;
    const int xcd = blockIdx.x & 7, idx = blockIdx.x >> 3;
    const int m0 = (xcd * 4 + idx / 8) * TM, n0 = (idx % 8) * TN;
    STAMP(5, 0);
    if (wave8 >= 2) {
        const int lw = wave8 - 2;
        int goff[GLW];
#pragma unroll
        for (int q = 0; q < GLW; ++q) {
            const int blk = lw * GLW + q;
            const int rowl = 8 * (blk < 8 ? blk : blk - 8) + (lane >> 3);
            const int c = (lane & 7) ^ ((rowl >> 1) & 7);
            goff[q] = (blk < 8 ? (m0 + rowl) : (n0 + rowl)) * kF + 8 * c;
        }
#define GLDS2(BUF, KTILE)                                                                            \
    do {                                                                                             \
        char* sa_ = shm + (BUF) * STAGE_B;                                                           \
        _Pragma("unroll") for (int q_ = 0; q_ < GLW; ++q_) {                                         \
            const int blk_ = lw * GLW + q_;                                                          \
            const bf16_t* src_ = (blk_ < 8 ? A : Bt) + goff[q_] + (KTILE) * BK;                      \
            __builtin_amdgcn_global_load_lds((const unsigned*)src_, (unsigned*)(sa_ + blk_ * 1024), 16, 0, 0); \
        }                                                                                            \
    } while (0)
#pragma unroll
        for (int pt = 0; pt < D; ++pt) GLDS2(pt, pt);
        WAIT_V((D - 1) * GLW);
        RAW_BARRIER();
        for (int kt = 0; kt < KT; ++kt) {
            if (kt + D < KT) {
                GLDS2((kt + D) % NBUF, kt + D);
                WAIT_V((D - 1) * GLW);
            } else {
                WAIT_V(0);
            }
            RAW_BARRIER();
        }
#undef GLDS2
    } else {
        const int w = wave8;
        const int r16 = lane & 15, q4 = lane >> 4;
        const int sw = (r16 >> 1) & 7;
        f32x4v acc[2][5];
#pragma unroll
        for (int i = 0; i < 2; ++i)
#pragma unroll
            for (int j = 0; j < 5; ++j) acc[i][j] = (f32x4v){0.f, 0.f, 0.f, 0.f};
        u32x2 bb[5];
#pragma unroll
        for (int ni = 0; ni < 5; ++ni) bb[ni] = *reinterpret_cast<const u32x2*>(bias + n0 + 16 * ni + 4 * q4);
        RAW_BARRIER();
        STAMP(5, 1);
        for (int kt = 0; kt < KT; ++kt) {
            const char* sa = shm + (kt % NBUF) * STAGE_B;
            const char* sb = sa + TM * 128;
            bf16x8 af[2][2], bfv[2][5];
#pragma unroll
            for (int ks = 0; ks < 2; ++ks) {
                const int coff = 16 * ((4 * ks + q4) ^ sw);
#pragma unroll
                for (int mi = 0; mi < 2; ++mi)
                    af[ks][mi] = *reinterpret_cast<const bf16x8*>(sa + (32 * w + 16 * mi + r16) * 128 + coff);
#pragma unroll
                for (int ni = 0; ni < 5; ++ni)
                    bfv[ks][ni] = *reinterpret_cast<const bf16x8*>(sb + (16 * ni + r16) * 128 + coff);
            }
#pragma unroll
            for (int ks = 0; ks < 2; ++ks)
#pragma unroll
                for (int mi = 0; mi < 2; ++mi)
#pragma unroll
                    for (int ni = 0; ni < 5; ++ni)
                        acc[mi][ni] = __builtin_amdgcn_mfma_f32_16x16x32_bf16(bfv[ks][ni], af[ks][mi], acc[mi][ni], 0, 0, 0);
            RAW_BARRIER();
        }
        STAMP(5, 2);
#pragma unroll
        for (int ni = 0; ni < 5; ++ni) {
            const float b0 = bf2f((bf16_t)(bb[ni][0] & 0xffffu)), b1 = bf2f((bf16_t)(bb[ni][0] >> 16));
            const float b2 = bf2f((bf16_t)(bb[ni][1] & 0xffffu)), b3 = bf2f((bf16_t)(bb[ni][1] >> 16));
#pragma unroll
            for (int mi = 0; mi < 2; ++mi) {
                u32x2 pk;
                pk[0] = pack2bf(rbf(acc[mi][ni][0]) + b0, rbf(acc[mi][ni][1]) + b1);
                pk[1] = pack2bf(rbf(acc[mi][ni][2]) + b2, rbf(acc[mi][ni][3]) + b3);
                *reinterpret_cast<u32x2*>(shm + (32 * w + 16 * mi + r16) * SSTR + (16 * ni + 4 * q4) * 2) = pk;
            }
        }
    }
    RAW_BARRIER();
    constexpr int CPR = TN / 8;
    for (int u = tid; u < TM * CPR; u += 512) {
        const int row = u / CPR, ch = u % CPR;
        const u32x4 v = *reinterpret_cast<const u32x4*>(shm + row * SSTR + 16 * ch);
        const size_t go = (size_t)(m0 + row) * kD + n0 + 8 * ch;
        const u32x4 hr = *reinterpret_cast<const u32x4*>(resid + go);
        u32x4 o;
#pragma unroll
        for (int q = 0; q < 4; ++q) {
            const float lo = bf2f((bf16_t)(hr[q] & 0xffffu)) + bf2f((bf16_t)(v[q] & 0xffffu));
            const float hi = bf2f((bf16_t)(hr[q] >> 16)) + bf2f((bf16_t)(v[q] >> 16));
            o[q] = pack2bf(lo, hi);
        }
        *reinterpret_cast<u32x4*>(C + go) = o;
    }
    STAMP(5, 3);
}

extern "C" void kernel_launch(void* const* d_in, const int* in_sizes, int n_in,
                              void* d_out, int out_size, void* d_ws, size_t ws_size,
                              hipStream_t stream) {
    (void)in_sizes; (void)n_in; (void)out_size; (void)ws_size;
    const bf16_t* A_x  = (const bf16_t*)d_in[0];
    const bf16_t* A_wr = (const bf16_t*)d_in[1];
    const bf16_t* A_wi = (const bf16_t*)d_in[2];
    const bf16_t* B_w1 = (const bf16_t*)d_in[4];
    const bf16_t* B_b1 = (const bf16_t*)d_in[5];
    const bf16_t* B_w2 = (const bf16_t*)d_in[6];
    const bf16_t* B_b2 = (const bf16_t*)d_in[7];
    bf16_t* out = (bf16_t*)d_out;

    char* ws = (char*)d_ws;
    float2* T      = (float2*)(ws + OFF_T);
    float2* T2     = (float2*)(ws + OFF_T2);
    bf16_t* hbuf   = (bf16_t*)(ws + OFF_H);
    bf16_t* yln2   = (bf16_t*)(ws + OFF_YLN2);
    bf16_t* Hbuf   = (bf16_t*)(ws + OFF_G);
    bf16_t* W1t    = (bf16_t*)(ws + OFF_W1T);
    bf16_t* W2t    = (bf16_t*)(ws + OFF_W2T);
    float2* tw2048 = (float2*)(ws + OFF_TW2048);
    float2* tw640  = (float2*)(ws + OFF_TW640);
    unsigned* WT   = (unsigned*)(ws + OFF_WT);

    ln_rfft_kernel<<<kNBlkA, 512, 0, stream>>>(A_x, T, B_w1, B_w2, W1t, W2t, A_wr, A_wi, WT, tw2048, tw640);
    colfft_kernel<<<kDF, 256, 0, stream>>>(T, T2, WT, tw2048);
    irfft_ln_kernel<<<kS / kRowsWG, 512, 0, stream>>>(A_x, T2, tw640, hbuf, yln2);
    gemm_kernel<256, 64, 4, 1, 3, 0, kF / 64><<<(kF / 64) * (kS / 256), 768, 0, stream>>>(
        yln2, kD, W1t, kD, kD, B_b1, nullptr, Hbuf, kF);
    gemm2_kernel<<<256, 512, 0, stream>>>(Hbuf, W2t, B_b2, hbuf, out);
}
```

```cpp
#include <hip/hip_runtime.h>
#include <hip/hip_bf16.h>
#include <stdint.h>

static constexpr int kS  = 2048;
static constexpr int kD  = 640;
static constexpr int kDF = 321;
static constexpr int kF  = 1728;
static constexpr float kLnEps = 1e-5f;

static constexpr size_t OFF_T     = 0;
static constexpr size_t OFF_T2    = OFF_T   + (size_t)kDF * kS * 8;
static constexpr size_t OFF_H     = OFF_T2  + (size_t)kDF * kS * 8;
static constexpr size_t OFF_YLN2  = OFF_H   + (size_t)kS * kD * 2;
static constexpr size_t OFF_G     = OFF_YLN2+ (size_t)kS * kD * 2;
static constexpr size_t OFF_W1T   = OFF_G   + (size_t)kS * kF * 2;
static constexpr size_t OFF_W2T   = OFF_W1T + (size_t)kF * kD * 2;
static constexpr size_t OFF_TW2048= OFF_W2T + (size_t)kD * kF * 2;
static constexpr size_t OFF_TW640 = OFF_TW2048 + (size_t)2048 * 8;
static constexpr size_t OFF_WT    = OFF_TW640 + (size_t)640 * 8;

typedef unsigned short bf16_t;

#define STAMP(KID, I) do { } while (0)

__device__ __forceinline__ float bf2f(bf16_t u) {
    return __uint_as_float(((unsigned)u) << 16);
}
__device__ __forceinline__ bf16_t f2bf(float f) {
    return __builtin_bit_cast(bf16_t, (__bf16)f);
}
typedef __attribute__((ext_vector_type(2))) __bf16 bf16x2_t;
typedef __attribute__((ext_vector_type(2))) float f32x2_t;
__device__ __forceinline__ unsigned pack2bf(float lo, float hi) {
    f32x2_t v = {lo, hi};
    bf16x2_t b = __builtin_convertvector(v, bf16x2_t);
    return __builtin_bit_cast(unsigned, b);
}
__device__ __forceinline__ float rbf(float f) { return bf2f(f2bf(f)); }

__device__ __forceinline__ float2 cadd(float2 a, float2 b) { return make_float2(a.x + b.x, a.y + b.y); }
__device__ __forceinline__ float2 csub(float2 a, float2 b) { return make_float2(a.x - b.x, a.y - b.y); }
__device__ __forceinline__ float2 cmul(float2 a, float2 b) {
    return make_float2(__fmaf_rn(a.x, b.x, -a.y * b.y), __fmaf_rn(a.x, b.y, a.y * b.x));
}
__device__ __forceinline__ float2 cmulc(float2 a, float2 b) {
    return make_float2(__fmaf_rn(a.x, b.x, a.y * b.y), __fmaf_rn(a.y, b.x, -a.x * b.y));
}
template <bool INV>
__device__ __forceinline__ float2 cmul_j(float2 a) {
    return INV ? make_float2(-a.y, a.x) : make_float2(a.y, -a.x);
}

template <bool INV>
__device__ __forceinline__ void dft4(float2 &b0, float2 &b1, float2 &b2, float2 &b3) {
    float2 t0 = cadd(b0, b2), t1 = csub(b0, b2), t2 = cadd(b1, b3), t3 = cmul_j<INV>(csub(b1, b3));
    b0 = cadd(t0, t2); b2 = csub(t0, t2); b1 = cadd(t1, t3); b3 = csub(t1, t3);
}
template <bool INV>
__device__ __forceinline__ void dft5(float2 a[5]) {
    const float c1 = 0.30901699437494745f, c2 = -0.8090169943749473f;
    const float s1 = 0.9510565162951535f,  s2 = 0.5877852522924731f;
    float2 b1 = cadd(a[1], a[4]), b4 = csub(a[1], a[4]);
    float2 b2 = cadd(a[2], a[3]), b3 = csub(a[2], a[3]);
    float2 t1 = make_float2(a[0].x + c1 * b1.x + c2 * b2.x, a[0].y + c1 * b1.y + c2 * b2.y);
    float2 t2 = make_float2(a[0].x + c2 * b1.x + c1 * b2.x, a[0].y + c2 * b1.y + c1 * b2.y);
    float2 u1 = make_float2(s1 * b4.x + s2 * b3.x, s1 * b4.y + s2 * b3.y);
    float2 u2 = make_float2(s2 * b4.x - s1 * b3.x, s2 * b4.y - s1 * b3.y);
    float2 iu1 = cmul_j<INV>(u1), iu2 = cmul_j<INV>(u2);
    a[0] = make_float2(a[0].x + b1.x + b2.x, a[0].y + b1.y + b2.y);
    a[1] = cadd(t1, iu1); a[4] = csub(t1, iu1);
    a[2] = cadd(t2, iu2); a[3] = csub(t2, iu2);
}
template <bool INV>
__device__ __forceinline__ void dft8(float2 a[8]) {
    const float r2 = 0.70710678118654752f;
    float2 e0 = a[0], e1 = a[2], e2 = a[4], e3 = a[6];
    float2 o0 = a[1], o1 = a[3], o2 = a[5], o3 = a[7];
    dft4<INV>(e0, e1, e2, e3);
    dft4<INV>(o0, o1, o2, o3);
    float2 p1, p3;
    if (!INV) {
        p1 = make_float2((o1.x + o1.y) * r2, (o1.y - o1.x) * r2);
        p3 = make_float2((o3.y - o3.x) * r2, -(o3.x + o3.y) * r2);
    } else {
        p1 = make_float2((o1.x - o1.y) * r2, (o1.x + o1.y) * r2);
        p3 = make_float2(-(o3.x + o3.y) * r2, (o3.x - o3.y) * r2);
    }
    float2 p2 = cmul_j<INV>(o2);
    a[0] = cadd(e0, o0); a[4] = csub(e0, o0);
    a[1] = cadd(e1, p1); a[5] = csub(e1, p1);
    a[2] = cadd(e2, p2); a[6] = csub(e2, p2);
    a[3] = cadd(e3, p3); a[7] = csub(e3, p3);
}

template <int R, bool INV> struct Dft;
template <bool INV> struct Dft<4, INV> {
    __device__ __forceinline__ static void run(float2 a[4]) { dft4<INV>(a[0], a[1], a[2], a[3]); }
};
template <bool INV> struct Dft<5, INV> {
    __device__ __forceinline__ static void run(float2 a[5]) { dft5<INV>(a); }
};
template <bool INV> struct Dft<8, INV> {
    __device__ __forceinline__ static void run(float2 a[8]) { dft8<INV>(a); }
};

template <int R, int NT, int NN, int SS, int NROWS, int TWSCALE, bool INV>
__device__ __forceinline__ void stockham_pass(const float2* __restrict__ in, float2* __restrict__ out,
                                              const float2* __restrict__ tw, int tid, int nthreads) {
    constexpr int M = NN / R;
    constexpr int NB = NT / R;
    for (int bt = tid; bt < NROWS * NB; bt += nthreads) {
        const int r   = bt / NB;
        const int rem = bt - r * NB;
        const int p   = rem / SS;
        const int q   = rem - p * SS;
        const float2* rin = in + r * NT;
        float2* rout = out + r * NT;
        float2 a[R];
#pragma unroll
        for (int k = 0; k < R; ++k) a[k] = rin[SS * (p + k * M) + q];
        Dft<R, INV>::run(a);
        if (NN != R) {
#pragma unroll
            for (int k = 1; k < R; ++k) {
                float2 w = tw[p * k * TWSCALE];
                a[k] = INV ? cmulc(a[k], w) : cmul(a[k], w);
            }
        }
#pragma unroll
        for (int k = 0; k < R; ++k) rout[SS * (R * p + k) + q] = a[k];
    }
}

static constexpr int kRowsWG = 8;
static constexpr int kNBlkFFT = kS / kRowsWG;
static constexpr int kNT1 = (kD / 64) * (kF / 64);
static constexpr int kNT2 = (kF / 64) * (kD / 64);
static constexpr int kNTW = (kS / 64) * ((kDF + 63) / 64);
static constexpr int kNTB = (2048 + 640 + 255) / 256;
static constexpr int kNBlkA = kNBlkFFT + kNT1 + kNT2 + kNTW + kNTB;

__device__ __forceinline__ void transpose_tile64(const bf16_t* __restrict__ src, bf16_t* __restrict__ dst,
                                                 int R, int C, int r0, int c0, bf16_t (*tile)[66], int tid) {
#pragma unroll
    for (int i = 0; i < 2; ++i) {
        int u = tid + 256 * i;
        int row = u >> 3, ch = u & 7;
        uint4 v = *reinterpret_cast<const uint4*>(src + (size_t)(r0 + row) * C + c0 + 8 * ch);
        const bf16_t* e = reinterpret_cast<const bf16_t*>(&v);
#pragma unroll
        for (int j = 0; j < 8; ++j) tile[row][8 * ch + j] = e[j];
    }
    __syncthreads();
#pragma unroll
    for (int i = 0; i < 2; ++i) {
        int u = tid + 256 * i;
        int orow = u >> 3, ch = u & 7;
        uint4 v;
        bf16_t* e = reinterpret_cast<bf16_t*>(&v);
#pragma unroll
        for (int j = 0; j < 8; ++j) e[j] = tile[8 * ch + j][orow];
        *reinterpret_cast<uint4*>(dst + (size_t)(c0 + orow) * R + r0 + 8 * ch) = v;
    }
}

__global__ void __launch_bounds__(512) ln_rfft_kernel(const bf16_t* __restrict__ x,
                                                      float2* __restrict__ T,
                                                      const bf16_t* __restrict__ W1, const bf16_t* __restrict__ W2,
                                                      bf16_t* __restrict__ W1t, bf16_t* __restrict__ W2t,
                                                      const bf16_t* __restrict__ w_r, const bf16_t* __restrict__ w_i,
                                                      unsigned* __restrict__ WT,
                                                      float2* __restrict__ tw2048_g, float2* __restrict__ tw640_g) {
    __shared__ float2 tw[640];
    __shared__ __attribute__((aligned(16))) float2 buf[2][kRowsWG * 320];
    const int tid = threadIdx.x;
    const int bid = blockIdx.x;
    if (bid >= kNBlkFFT) {
        if (tid >= 256) return;
        int b = bid - kNBlkFFT;
        if (b < kNT1) {
            bf16_t (*tile)[66] = reinterpret_cast<bf16_t (*)[66]>(&buf[0][0]);
            transpose_tile64(W1, W1t, kD, kF, (b / (kF / 64)) * 64, (b % (kF / 64)) * 64, tile, tid);
            return;
        }
        b -= kNT1;
        if (b < kNT2) {
            bf16_t (*tile)[66] = reinterpret_cast<bf16_t (*)[66]>(&buf[0][0]);
            transpose_tile64(W2, W2t, kF, kD, (b / (kD / 64)) * 64, (b % (kD / 64)) * 64, tile, tid);
            return;
        }
        b -= kNT2;
        if (b < kNTW) {
            unsigned (*tile)[65] = reinterpret_cast<unsigned (*)[65]>(&buf[0][0]);
            const int s0 = (b / 6) * 64, l0 = (b % 6) * 64;
            const int ll = tid & 63, sr = tid >> 6;
            if (l0 + ll < kDF) {
#pragma unroll
                for (int i = 0; i < 16; ++i) {
                    const int s = sr + 4 * i;
                    const size_t idx = (size_t)(s0 + s) * kDF + l0 + ll;
                    tile[s][ll] = (unsigned)w_r[idx] | ((unsigned)w_i[idx] << 16);
                }
            }
            __syncthreads();
            const int ss = tid & 63, lr = tid >> 6;
#pragma unroll
            for (int i = 0; i < 16; ++i) {
                const int l = lr + 4 * i;
                if (l0 + l < kDF) WT[(size_t)(l0 + l) * kS + s0 + ss] = tile[ss][l];
            }
            return;
        }
        b -= kNTW;
        {
            const int idx = b * 256 + tid;
            if (idx < 2048) {
                float s, c;
                sincospif(-2.0f * (float)idx / 2048.0f, &s, &c);
                tw2048_g[idx] = make_float2(c, s);
            } else if (idx < 2048 + 640) {
                float s, c;
                sincospif(-2.0f * (float)(idx - 2048) / 640.0f, &s, &c);
                tw640_g[idx - 2048] = make_float2(c, s);
            }
            return;
        }
    }
    const int row0 = bid * kRowsWG;
    STAMP(1, 0);
    const int r = tid >> 6, l64 = tid & 63;
    const bf16_t* xr = x + (size_t)(row0 + r) * kD;
    unsigned xv[5];
#pragma unroll
    for (int i = 0; i < 5; ++i) xv[i] = *reinterpret_cast<const unsigned*>(xr + 2 * (l64 + 64 * i));
    for (int i = tid; i < 640; i += 512) {
        float sn, cs;
        sincospif(-2.0f * (float)i / 640.0f, &sn, &cs);
        tw[i] = make_float2(cs, sn);
    }
    float v[10];
    float sum = 0.f;
#pragma unroll
    for (int i = 0; i < 5; ++i) {
        v[2 * i + 0] = bf2f((bf16_t)(xv[i] & 0xffffu)); v[2 * i + 1] = bf2f((bf16_t)(xv[i] >> 16));
        sum += v[2 * i] + v[2 * i + 1];
    }
#pragma unroll
    for (int off = 32; off > 0; off >>= 1) sum += __shfl_xor(sum, off);
    const float mean = sum * (1.0f / kD);
    const float mb = rbf(mean);
    float ss = 0.f;
#pragma unroll
    for (int i = 0; i < 10; ++i) { float d = v[i] - mean; ss += d * d; }
#pragma unroll
    for (int off = 32; off > 0; off >>= 1) ss += __shfl_xor(ss, off);
    const float var = ss * (1.0f / kD);
    const float den = rbf(sqrtf(rbf(rbf(var) + kLnEps)));
#pragma unroll
    for (int i = 0; i < 5; ++i)
        buf[0][r * 320 + l64 + 64 * i] = make_float2(rbf(rbf(v[2 * i] - mb) / den), rbf(rbf(v[2 * i + 1] - mb) / den));
    __syncthreads();
    STAMP(1, 1);
    stockham_pass<8, 320, 320, 1, kRowsWG, 2, false>(buf[0], buf[1], tw, tid, 512);
    __syncthreads();
    STAMP(1, 2);
    stockham_pass<8, 320, 40, 8, kRowsWG, 16, false>(buf[1], buf[0], tw, tid, 512);
    __syncthreads();
    STAMP(1, 3);
    stockham_pass<5, 320, 5, 64, kRowsWG, 128, false>(buf[0], buf[1], tw, tid, 512);
    __syncthreads();
    STAMP(1, 4);
    const float2* Z = buf[1];
    for (int task = tid; task < 2 * kDF; task += 512) {
        const int k = task >> 1, rh = (task & 1) * 4;
        const int km = (320 - k) % 320;
        const int kk = (k == 320) ? 0 : k;
        const float2 w = tw[k];
        float2 outv[4];
#pragma unroll
        for (int rr = 0; rr < 4; ++rr) {
            const float2 Ze = Z[(rh + rr) * 320 + kk];
            const float2 Zm = Z[(rh + rr) * 320 + km];
            const float2 E = make_float2(0.5f * (Ze.x + Zm.x), 0.5f * (Ze.y - Zm.y));
            const float dx = Ze.x - Zm.x, dy = Ze.y + Zm.y;
            const float2 O = make_float2(0.5f * dy, -0.5f * dx);
            outv[rr] = cadd(E, cmul(w, O));
        }
        float4* dst = reinterpret_cast<float4*>(T + (size_t)k * kS + row0 + rh);
        dst[0] = make_float4(outv[0].x, outv[0].y, outv[1].x, outv[1].y);
        dst[1] = make_float4(outv[2].x, outv[2].y, outv[3].x, outv[3].y);
    }
    STAMP(1, 5);
}

__global__ void __launch_bounds__(256) colfft_kernel(const float2* __restrict__ T,
                                                     float2* __restrict__ T2,
                                                     const unsigned* __restrict__ WT,
                                                     const float2* __restrict__ tw2048_g) {
    __shared__ __attribute__((aligned(16))) float2 tw[2048];
    __shared__ __attribute__((aligned(16))) float2 buf[2][2048];
    const int tid = threadIdx.x;
    const int l = blockIdx.x;
    STAMP(2, 0);
    unsigned wp[8];
#pragma unroll
    for (int j = 0; j < 8; ++j) wp[j] = WT[(size_t)l * kS + tid + 256 * j];
    const float4* tw4 = reinterpret_cast<const float4*>(tw2048_g);
    float4* twd = reinterpret_cast<float4*>(tw);
    const float4* col4 = reinterpret_cast<const float4*>(T + (size_t)l * kS);
    float4* b04 = reinterpret_cast<float4*>(buf[0]);
    float4 tv[4], cv[4];
#pragma unroll
    for (int i = 0; i < 4; ++i) { tv[i] = tw4[tid + 256 * i]; cv[i] = col4[tid + 256 * i]; }
#pragma unroll
    for (int i = 0; i < 4; ++i) { twd[tid + 256 * i] = tv[i]; b04[tid + 256 * i] = cv[i]; }
    __syncthreads();
    STAMP(2, 1);
    stockham_pass<8, 2048, 2048, 1, 1, 1, false>(buf[0], buf[1], tw, tid, 256);
    __syncthreads();
    STAMP(2, 2);
    stockham_pass<8, 2048, 256, 8, 1, 8, false>(buf[1], buf[0], tw, tid, 256);
    __syncthreads();
    STAMP(2, 3);
    stockham_pass<8, 2048, 32, 64, 1, 64, false>(buf[0], buf[1], tw, tid, 256);
    __syncthreads();
    STAMP(2, 4);
    {
        const float sc = 1.0f / 2048.0f;
#pragma unroll
        for (int t2 = 0; t2 < 2; ++t2) {
            const int q = tid + 256 * t2;
            float2 a[4];
#pragma unroll
            for (int k = 0; k < 4; ++k) a[k] = buf[1][512 * k + q];
            dft4<false>(a[0], a[1], a[2], a[3]);
#pragma unroll
            for (int k = 0; k < 4; ++k) {
                const int j = 2 * k + t2;
                const float2 wc = make_float2(bf2f((bf16_t)(wp[j] & 0xffffu)) * sc, bf2f((bf16_t)(wp[j] >> 16)) * sc);
                buf[0][512 * k + q] = cmul(a[k], wc);
            }
        }
    }
    __syncthreads();
    STAMP(2, 5);
    stockham_pass<8, 2048, 2048, 1, 1, 1, true>(buf[0], buf[1], tw, tid, 256);
    __syncthreads();
    STAMP(2, 6);
    stockham_pass<8, 2048, 256, 8, 1, 8, true>(buf[1], buf[0], tw, tid, 256);
    __syncthreads();
    STAMP(2, 7);
    stockham_pass<8, 2048, 32, 64, 1, 64, true>(buf[0], buf[1], tw, tid, 256);
    __syncthreads();
    STAMP(2, 8);
#pragma unroll
    for (int t2 = 0; t2 < 2; ++t2) {
        const int q = tid + 256 * t2;
        float2 a[4];
#pragma unroll
        for (int k = 0; k < 4; ++k) a[k] = buf[1][512 * k + q];
        dft4<true>(a[0], a[1], a[2], a[3]);
#pragma unroll
        for (int k = 0; k < 4; ++k) T2[(size_t)l * kS + 512 * k + q] = a[k];
    }
    STAMP(2, 9);
}

__global__ void __launch_bounds__(512) irfft_ln_kernel(const bf16_t* __restrict__ x,
                                                       const float2* __restrict__ T2,
                                                       const float2* __restrict__ tw640_g,
                                                       bf16_t* __restrict__ hout,
                                                       bf16_t* __restrict__ yln2) {
    __shared__ float2 tw[640];
    __shared__ __attribute__((aligned(16))) float2 X[kRowsWG][322];
    __shared__ __attribute__((aligned(16))) float2 buf[2][kRowsWG * 320];
    const int tid = threadIdx.x;
    const int row0 = (((blockIdx.x & 7) * 32) + (blockIdx.x >> 3)) * kRowsWG;
    STAMP(3, 0);
    const int r = tid >> 6, l64 = tid & 63;
    const bf16_t* xrow = x + (size_t)(row0 + r) * kD;
    unsigned xv[5];
#pragma unroll
    for (int i = 0; i < 5; ++i) xv[i] = *reinterpret_cast<const unsigned*>(xrow + 2 * (l64 + 64 * i));
    for (int i = tid; i < 640; i += 512) tw[i] = tw640_g[i];
    for (int task = tid; task < 2 * kDF; task += 512) {
        const int k = task >> 1, rh = (task & 1) * 4;
        const float4* src = reinterpret_cast<const float4*>(T2 + (size_t)k * kS + row0 + rh);
        const float4 v0 = src[0], v1 = src[1];
        X[rh + 0][k] = make_float2(v0.x, v0.y); X[rh + 1][k] = make_float2(v0.z, v0.w);
        X[rh + 2][k] = make_float2(v1.x, v1.y); X[rh + 3][k] = make_float2(v1.z, v1.w);
    }
    __syncthreads();
    STAMP(3, 1);
    if (tid < kRowsWG * 40) {
        const int rr = tid / 40, p = tid - rr * 40;
        float2 a[8];
#pragma unroll
        for (int k = 0; k < 8; ++k) {
            const int j = p + 40 * k;
            float2 Xk = X[rr][j];
            float2 Xm = X[rr][320 - j];
            if (j == 0) { Xk.y = 0.f; Xm.y = 0.f; }
            const float2 E = make_float2(0.5f * (Xk.x + Xm.x), 0.5f * (Xk.y - Xm.y));
            const float2 d = make_float2(Xk.x - Xm.x, Xk.y + Xm.y);
            float2 O = cmulc(d, tw[j]);
            O.x *= 0.5f; O.y *= 0.5f;
            a[k] = make_float2(E.x - O.y, E.y + O.x);
        }
        dft8<true>(a);
#pragma unroll
        for (int k = 1; k < 8; ++k) a[k] = cmulc(a[k], tw[2 * p * k]);
        float2* rout = buf[1] + rr * 320;
#pragma unroll
        for (int k = 0; k < 8; ++k) rout[8 * p + k] = a[k];
    }
    __syncthreads();
    STAMP(3, 3);
    stockham_pass<8, 320, 40, 8, kRowsWG, 16, true>(buf[1], buf[0], tw, tid, 512);
    __syncthreads();
    STAMP(3, 4);
    stockham_pass<5, 320, 5, 64, kRowsWG, 128, true>(buf[0], buf[1], tw, tid, 512);
    __syncthreads();
    STAMP(3, 5);
    const float2* zr = buf[1] + r * 320;
    bf16_t* hrow = hout + (size_t)(row0 + r) * kD;
    bf16_t* yrow = yln2 + (size_t)(row0 + r) * kD;
    float hv[10];
    float sum = 0.f;
#pragma unroll
    for (int i = 0; i < 5; ++i) {
        const int c = l64 + 64 * i;
        const float2 zv = zr[c];
        const float h0 = rbf(bf2f((bf16_t)(xv[i] & 0xffffu)) + rbf(zv.x * (1.0f / 320.0f)));
        const float h1 = rbf(bf2f((bf16_t)(xv[i] >> 16)) + rbf(zv.y * (1.0f / 320.0f)));
        hv[2 * i] = h0; hv[2 * i + 1] = h1;
        *reinterpret_cast<unsigned*>(hrow + 2 * c) = pack2bf(h0, h1);
        sum += h0 + h1;
    }
#pragma unroll
    for (int off = 32; off > 0; off >>= 1) sum += __shfl_xor(sum, off);
    const float mean = sum * (1.0f / kD);
    const float mb = rbf(mean);
    float ss = 0.f;
#pragma unroll
    for (int i = 0; i < 10; ++i) { float d = hv[i] - mean; ss += d * d; }
#pragma unroll
    for (int off = 32; off > 0; off >>= 1) ss += __shfl_xor(ss, off);
    const float var = ss * (1.0f / kD);
    const float den = rbf(sqrtf(rbf(rbf(var) + kLnEps)));
    float y1[10];
    float sum2 = 0.f;
#pragma unroll
    for (int i = 0; i < 10; ++i) { y1[i] = rbf(rbf(hv[i] - mb) / den); sum2 += y1[i]; }
#pragma unroll
    for (int off = 32; off > 0; off >>= 1) sum2 += __shfl_xor(sum2, off);
    const float mean2 = sum2 * (1.0f / kD);
    const float mb2 = rbf(mean2);
    float ss2 = 0.f;
#pragma unroll
    for (int i = 0; i < 10; ++i) { float d = y1[i] - mean2; ss2 += d * d; }
#pragma unroll
    for (int off = 32; off > 0; off >>= 1) ss2 += __shfl_xor(ss2, off);
    const float var2 = ss2 * (1.0f / kD);
    const float den2 = rbf(sqrtf(rbf(rbf(var2) + kLnEps)));
#pragma unroll
    for (int i = 0; i < 5; ++i) {
        const int c = l64 + 64 * i;
        *reinterpret_cast<unsigned*>(yrow + 2 * c) = pack2bf(rbf(y1[2 * i] - mb2) / den2, rbf(y1[2 * i + 1] - mb2) / den2);
    }
    STAMP(3, 6);
}

using bf16x8 = __attribute__((ext_vector_type(8))) __bf16;
using f32x16 = __attribute__((ext_vector_type(16))) float;
typedef unsigned __attribute__((ext_vector_type(4))) u32x4;
typedef unsigned __attribute__((ext_vector_type(2))) u32x2;

#define WAIT_V(n) asm volatile("s_waitcnt vmcnt(%0)" ::"n"(n) : "memory")
#define WAIT_L(n) asm volatile("s_waitcnt lgkmcnt(%0)" ::"n"(n) : "memory")
#define RAW_BARRIER() do { WAIT_L(0); __builtin_amdgcn_s_barrier(); } while (0)

__device__ __forceinline__ float gelu_tanh_f(float t) {
    const float x2 = t * __fmaf_rn(0.0713548162726008f, t * t, 1.5957691216057308f);
    return t * __frcp_rn(1.0f + __expf(-x2));
}

template <int TM, int TN, int WM, int WN, int NBUF, int EPI, int NTN>
__global__ void __launch_bounds__(768) gemm_kernel(const bf16_t* __restrict__ A, int lda,
                                                   const bf16_t* __restrict__ Bt, int ldb, int K,
                                                   const bf16_t* __restrict__ bias,
                                                   const bf16_t* __restrict__ resid,
                                                   bf16_t* __restrict__ C, int ldc) {
    constexpr int BK = 64;
    constexpr int D = NBUF - 1;
    constexpr int WTM = TM / WM, WTN = TN / WN;
    constexpr int MB = WTM / 32, NB = WTN / 32;
    constexpr int A_TILE_B = TM * 128, B_TILE_B = TN * 128, STAGE_B = A_TILE_B + B_TILE_B;
    constexpr int NLW = 8;
    constexpr int GA = TM / 8 / NLW, GB = TN / 8 / NLW;
    constexpr int GL = GA + GB;
    static_assert(GA >= 1 && GB >= 1, "loader split");
    static_assert(WM * WN == 4, "4 compute waves");
    static_assert((D - 1) * GL <= 63, "vmcnt range");
    constexpr int SSTR = TN * 2 + 16;
    static_assert(TM * SSTR <= NBUF * STAGE_B, "epilogue staging fits");
    __shared__ __attribute__((aligned(1024))) char shm[NBUF * STAGE_B];

    const int tid = threadIdx.x, lane = tid & 63, wave8 = tid >> 6;
    const bool loader = wave8 >= 4;
    const int wave = loader ? (wave8 - 4) : wave8;
    const int wm = wave / WN, wn = wave % WN;
    constexpr int MPX = (kS / TM) / 8;
    const int xcd = blockIdx.x & 7, idx = blockIdx.x >> 3;
    const int m0 = (xcd * MPX + idx / NTN) * TM, n0 = (idx % NTN) * TN;
    const int r32 = lane & 31, hh = lane >> 5;
    const int KT = K / BK;
    STAMP(4 + EPI, 0);

    if (loader) {
        int aoff[GA], boff[GB];
#pragma unroll
        for (int i = 0; i < GA; ++i) {
            const int row = 8 * (wave * GA + i) + (lane >> 3);
            const int c = (lane & 7) ^ ((row >> 1) & 7);
            aoff[i] = (m0 + row) * lda + 8 * c;
        }
#pragma unroll
        for (int i = 0; i < GB; ++i) {
            const int row = 8 * (wave * GB + i) + (lane >> 3);
            const int c = (lane & 7) ^ ((row >> 1) & 7);
            boff[i] = (n0 + row) * ldb + 8 * c;
        }
#define GLDS_STAGE(BUF, KTILE)                                                                      \
    do {                                                                                            \
        char* sa_ = shm + (BUF) * STAGE_B;                                                          \
        _Pragma("unroll") for (int q_ = 0; q_ < GA; ++q_)                                           \
            __builtin_amdgcn_global_load_lds((const unsigned*)(A + aoff[q_] + (KTILE) * BK),        \
                                             (unsigned*)(sa_ + (wave * GA + q_) * 1024), 16, 0, 0); \
        _Pragma("unroll") for (int q_ = 0; q_ < GB; ++q_)                                           \
            __builtin_amdgcn_global_load_lds((const unsigned*)(Bt + boff[q_] + (KTILE) * BK),       \
                                             (unsigned*)(sa_ + A_TILE_B + (wave * GB + q_) * 1024), 16, 0, 0); \
    } while (0)
#pragma unroll
        for (int pt = 0; pt < D; ++pt) GLDS_STAGE(pt, pt);
        WAIT_V((D - 1) * GL);
        RAW_BARRIER();
        for (int kt = 0; kt < KT; ++kt) {
            if (kt + D < KT) {
                GLDS_STAGE((kt + D) % NBUF, kt + D);
                WAIT_V((D - 1) * GL);
            } else {
                WAIT_V(0);
            }
            RAW_BARRIER();
        }
#undef GLDS_STAGE
    } else {
    f32x16 acc[MB][NB];
#pragma unroll
    for (int i = 0; i < MB; ++i)
#pragma unroll
        for (int j = 0; j < NB; ++j)
#pragma unroll
            for (int e = 0; e < 16; ++e) acc[i][j][e] = 0.f;
    const int sw = (r32 >> 1) & 7;
    u32x2 bb[NB][4];
#pragma unroll
    for (int ni = 0; ni < NB; ++ni)
#pragma unroll
        for (int g = 0; g < 4; ++g)
            bb[ni][g] = *reinterpret_cast<const u32x2*>(bias + n0 + wn * WTN + ni * 32 + 8 * g + 4 * hh);
    RAW_BARRIER();
    STAMP(4 + EPI, 1);
    for (int kt = 0; kt < KT; ++kt) {
        const char* sa = shm + (kt % NBUF) * STAGE_B;
        const char* sb = sa + A_TILE_B;
        bf16x8 af[4][MB], bfv[4][NB];
#pragma unroll
        for (int kk = 0; kk < 4; ++kk) {
            const int coff = 16 * ((2 * kk + hh) ^ sw);
#pragma unroll
            for (int mi = 0; mi < MB; ++mi)
                af[kk][mi] = *reinterpret_cast<const bf16x8*>(sa + (wm * WTM + mi * 32 + r32) * 128 + coff);
#pragma unroll
            for (int ni = 0; ni < NB; ++ni)
                bfv[kk][ni] = *reinterpret_cast<const bf16x8*>(sb + (wn * WTN + ni * 32 + r32) * 128 + coff);
        }
#pragma unroll
        for (int kk = 0; kk < 4; ++kk)
#pragma unroll
            for (int mi = 0; mi < MB; ++mi)
#pragma unroll
                for (int ni = 0; ni < NB; ++ni)
                    acc[mi][ni] = __builtin_amdgcn_mfma_f32_32x32x16_bf16(bfv[kk][ni], af[kk][mi], acc[mi][ni], 0, 0, 0);
        RAW_BARRIER();
    }
    STAMP(4 + EPI, 2);
#pragma unroll
    for (int ni = 0; ni < NB; ++ni) {
#pragma unroll
        for (int g = 0; g < 4; ++g) {
            const int nl = ni * 32 + 8 * g + 4 * hh;
            const float b0 = bf2f((bf16_t)(bb[ni][g][0] & 0xffffu)), b1 = bf2f((bf16_t)(bb[ni][g][0] >> 16));
            const float b2 = bf2f((bf16_t)(bb[ni][g][1] & 0xffffu)), b3 = bf2f((bf16_t)(bb[ni][g][1] >> 16));
#pragma unroll
            for (int mi = 0; mi < MB; ++mi) {
                const float t0 = rbf(acc[mi][ni][4 * g + 0]) + b0;
                const float t1 = rbf(acc[mi][ni][4 * g + 1]) + b1;
                const float t2 = rbf(acc[mi][ni][4 * g + 2]) + b2;
                const float t3 = rbf(acc[mi][ni][4 * g + 3]) + b3;
                u32x2 pk;
                pk[0] = pack2bf(t0, t1);
                pk[1] = pack2bf(t2, t3);
                *reinterpret_cast<u32x2*>(shm + (wm * WTM + mi * 32 + r32) * SSTR + (wn * WTN + nl) * 2) = pk;
            }
        }
    }
    }
    RAW_BARRIER();
    constexpr int CPR = TN / 8;
    for (int u = tid; u < TM * CPR; u += 768) {
        const int row = u / CPR, ch = u % CPR;
        const u32x4 v = *reinterpret_cast<const u32x4*>(shm + row * SSTR + 16 * ch);
        const size_t go = (size_t)(m0 + row) * ldc + n0 + 8 * ch;
        u32x4 o;
        if (EPI == 0) {
#pragma unroll
            for (int q = 0; q < 4; ++q)
                o[q] = pack2bf(gelu_tanh_f(bf2f((bf16_t)(v[q] & 0xffffu))), gelu_tanh_f(bf2f((bf16_t)(v[q] >> 16))));
        } else {
            const u32x4 hr = *reinterpret_cast<const u32x4*>(resid + go);
#pragma unroll
            for (int q = 0; q < 4; ++q) {
                const float lo = bf2f((bf16_t)(hr[q] & 0xffffu)) + bf2f((bf16_t)(v[q] & 0xffffu));
                const float hi = bf2f((bf16_t)(hr[q] >> 16)) + bf2f((bf16_t)(v[q] >> 16));
                o[q] = pack2bf(lo, hi);
            }
        }
        *reinterpret_cast<u32x4*>(C + go) = o;
    }
    STAMP(4 + EPI, 3);
}

using f32x4v = __attribute__((ext_vector_type(4))) float;

__global__ void __launch_bounds__(512) gemm2_kernel(const bf16_t* __restrict__ A, const bf16_t* __restrict__ Bt,
                                                    const bf16_t* __restrict__ bias,
                                                    const bf16_t* __restrict__ resid,
                                                    bf16_t* __restrict__ C) {
    constexpr int TM = 64, TN = 80, BK = 64, KT = kF / BK;
    constexpr int NBUF = 6, D = NBUF - 1;
    constexpr int STAGE_B = (TM + TN) * 128;
    constexpr int GLW = 3;
    constexpr int SSTR = TN * 2 + 16;
    static_assert((D - 1) * GLW <= 63, "vmcnt range");
    static_assert(TM * SSTR <= NBUF * STAGE_B, "staging fits");
    __shared__ __attribute__((aligned(1024))) char shm[NBUF * STAGE_B];

    const int tid = threadIdx.x, lane = tid & 63, wave8 = tid >> 6;
    const int xcd = blockIdx.x & 7, idx = blockIdx.x >> 3;
    const int m0 = (xcd * 4 + idx / 8) * TM, n0 = (idx % 8) * TN;
    STAMP(5, 0);
    if (wave8 >= 2) {
        const int lw = wave8 - 2;
        int goff[GLW];
#pragma unroll
        for (int q = 0; q < GLW; ++q) {
            const int blk = lw * GLW + q;
            const int rowl = 8 * (blk < 8 ? blk : blk - 8) + (lane >> 3);
            const int c = (lane & 7) ^ ((rowl >> 1) & 7);
            goff[q] = (blk < 8 ? (m0 + rowl) : (n0 + rowl)) * kF + 8 * c;
        }
#define GLDS2(BUF, KTILE)                                                                            \
    do {                                                                                             \
        char* sa_ = shm + (BUF) * STAGE_B;                                                           \
        _Pragma("unroll") for (int q_ = 0; q_ < GLW; ++q_) {                                         \
            const int blk_ = lw * GLW + q_;                                                          \
            const bf16_t* src_ = (blk_ < 8 ? A : Bt) + goff[q_] + (KTILE) * BK;                      \
            __builtin_amdgcn_global_load_lds((const unsigned*)src_, (unsigned*)(sa_ + blk_ * 1024), 16, 0, 0); \
        }                                                                                            \
    } while (0)
#pragma unroll
        for (int pt = 0; pt < D; ++pt) GLDS2(pt, pt);
        WAIT_V((D - 1) * GLW);
        RAW_BARRIER();
        for (int kt = 0; kt < KT; ++kt) {
            if (kt + D < KT) {
                GLDS2((kt + D) % NBUF, kt + D);
                WAIT_V((D - 1) * GLW);
            } else {
                WAIT_V(0);
            }
            RAW_BARRIER();
        }
#undef GLDS2
    } else {
        const int w = wave8;
        const int r16 = lane & 15, q4 = lane >> 4;
        const int sw = (r16 >> 1) & 7;
        f32x4v acc[2][5];
#pragma unroll
        for (int i = 0; i < 2; ++i)
#pragma unroll
            for (int j = 0; j < 5; ++j) acc[i][j] = (f32x4v){0.f, 0.f, 0.f, 0.f};
        u32x2 bb[5];
#pragma unroll
        for (int ni = 0; ni < 5; ++ni) bb[ni] = *reinterpret_cast<const u32x2*>(bias + n0 + 16 * ni + 4 * q4);
        RAW_BARRIER();
        STAMP(5, 1);
        for (int kt = 0; kt < KT; ++kt) {
            const char* sa = shm + (kt % NBUF) * STAGE_B;
            const char* sb = sa + TM * 128;
            bf16x8 af[2][2], bfv[2][5];
#pragma unroll
            for (int ks = 0; ks < 2; ++ks) {
                const int coff = 16 * ((4 * ks + q4) ^ sw);
#pragma unroll
                for (int mi = 0; mi < 2; ++mi)
                    af[ks][mi] = *reinterpret_cast<const bf16x8*>(sa + (32 * w + 16 * mi + r16) * 128 + coff);
#pragma unroll
                for (int ni = 0; ni < 5; ++ni)
                    bfv[ks][ni] = *reinterpret_cast<const bf16x8*>(sb + (16 * ni + r16) * 128 + coff);
            }
#pragma unroll
            for (int ks = 0; ks < 2; ++ks)
#pragma unroll
                for (int mi = 0; mi < 2; ++mi)
#pragma unroll
                    for (int ni = 0; ni < 5; ++ni)
                        acc[mi][ni] = __builtin_amdgcn_mfma_f32_16x16x32_bf16(bfv[ks][ni], af[ks][mi], acc[mi][ni], 0, 0, 0);
            RAW_BARRIER();
        }
        STAMP(5, 2);
#pragma unroll
        for (int ni = 0; ni < 5; ++ni) {
            const float b0 = bf2f((bf16_t)(bb[ni][0] & 0xffffu)), b1 = bf2f((bf16_t)(bb[ni][0] >> 16));
            const float b2 = bf2f((bf16_t)(bb[ni][1] & 0xffffu)), b3 = bf2f((bf16_t)(bb[ni][1] >> 16));
#pragma unroll
            for (int mi = 0; mi < 2; ++mi) {
                u32x2 pk;
                pk[0] = pack2bf(rbf(acc[mi][ni][0]) + b0, rbf(acc[mi][ni][1]) + b1);
                pk[1] = pack2bf(rbf(acc[mi][ni][2]) + b2, rbf(acc[mi][ni][3]) + b3);
                *reinterpret_cast<u32x2*>(shm + (32 * w + 16 * mi + r16) * SSTR + (16 * ni + 4 * q4) * 2) = pk;
            }
        }
    }
    RAW_BARRIER();
    constexpr int CPR = TN / 8;
    for (int u = tid; u < TM * CPR; u += 512) {
        const int row = u / CPR, ch = u % CPR;
        const u32x4 v = *reinterpret_cast<const u32x4*>(shm + row * SSTR + 16 * ch);
        const size_t go = (size_t)(m0 + row) * kD + n0 + 8 * ch;
        const u32x4 hr = *reinterpret_cast<const u32x4*>(resid + go);
        u32x4 o;
#pragma unroll
        for (int q = 0; q < 4; ++q) {
            const float lo = bf2f((bf16_t)(hr[q] & 0xffffu)) + bf2f((bf16_t)(v[q] & 0xffffu));
            const float hi = bf2f((bf16_t)(hr[q] >> 16)) + bf2f((bf16_t)(v[q] >> 16));
            o[q] = pack2bf(lo, hi);
        }
        *reinterpret_cast<u32x4*>(C + go) = o;
    }
    STAMP(5, 3);
}

__global__ void __launch_bounds__(640) gemm1_kernel(const bf16_t* __restrict__ A, const bf16_t* __restrict__ Bt,
                                                    const bf16_t* __restrict__ bias,
                                                    bf16_t* __restrict__ C) {
    constexpr int TM = 128, TNMAX = 112, BK = 64, KT = kD / BK;
    constexpr int NBUF = 5, D = NBUF - 1;
    constexpr int STAGE_B = (TM + TNMAX) * 128;
    constexpr int GLW = 5;
    constexpr int SSTR = TNMAX * 2 + 16;
    static_assert((D - 1) * GLW <= 63, "vmcnt range");
    static_assert(TM * SSTR <= NBUF * STAGE_B, "staging fits");
    __shared__ __attribute__((aligned(1024))) char shm[NBUF * STAGE_B];

    const int tid = threadIdx.x, lane = tid & 63, wave10 = tid >> 6;
    const int xcd = blockIdx.x & 7, idx = blockIdx.x >> 3;
    const int j = idx & 15;
    const int m0 = (xcd * 2 + (idx >> 4)) * TM;
    const int width = (j < 12) ? 112 : 96;
    const int n0 = (j < 12) ? 112 * j : 1344 + 96 * (j - 12);
    STAMP(4, 0);
    if (wave10 >= 4) {
        const int lw = wave10 - 4;
        int goff[GLW];
#pragma unroll
        for (int q = 0; q < GLW; ++q) {
            const int blk = lw * GLW + q;
            const int rowl = 8 * (blk < 16 ? blk : blk - 16) + (lane >> 3);
            const int c = (lane & 7) ^ ((rowl >> 1) & 7);
            const int grow = (blk < 16) ? (m0 + rowl) : (n0 + (rowl < width ? rowl : width - 1));
            goff[q] = grow * kD + 8 * c;
        }
#define GLDS1(BUF, KTILE)                                                                            \
    do {                                                                                             \
        char* sa_ = shm + (BUF) * STAGE_B;                                                           \
        _Pragma("unroll") for (int q_ = 0; q_ < GLW; ++q_) {                                         \
            const int blk_ = lw * GLW + q_;                                                          \
            const bf16_t* src_ = (blk_ < 16 ? A : Bt) + goff[q_] + (KTILE) * BK;                     \
            __builtin_amdgcn_global_load_lds((const unsigned*)src_, (unsigned*)(sa_ + blk_ * 1024), 16, 0, 0); \
        }                                                                                            \
    } while (0)
#pragma unroll
        for (int pt = 0; pt < D; ++pt) GLDS1(pt, pt);
        WAIT_V((D - 1) * GLW);
        RAW_BARRIER();
        for (int kt = 0; kt < KT; ++kt) {
            if (kt + D < KT) {
                GLDS1((kt + D) % NBUF, kt + D);
                WAIT_V((D - 1) * GLW);
            } else {
                WAIT_V(0);
            }
            RAW_BARRIER();
        }
#undef GLDS1
    } else {
        const int w = wave10;
        const int r16 = lane & 15, q4 = lane >> 4;
        const int sw = (r16 >> 1) & 7;
        const bool full = (width == 112);
        f32x4v acc[2][7];
#pragma unroll
        for (int i = 0; i < 2; ++i)
#pragma unroll
            for (int jj = 0; jj < 7; ++jj) acc[i][jj] = (f32x4v){0.f, 0.f, 0.f, 0.f};
        u32x2 bb[7];
#pragma unroll
        for (int ni = 0; ni < 7; ++ni)
            bb[ni] = (ni < 6 || full) ? *reinterpret_cast<const u32x2*>(bias + n0 + 16 * ni + 4 * q4) : (u32x2){0u, 0u};
        RAW_BARRIER();
        STAMP(4, 1);
        for (int kt = 0; kt < KT; ++kt) {
            const char* sa = shm + (kt % NBUF) * STAGE_B;
            const char* sb = sa + TM * 128;
#pragma unroll
            for (int ks = 0; ks < 2; ++ks) {
                const int coff = 16 * ((4 * ks + q4) ^ sw);
                bf16x8 af[2], bfv[7];
#pragma unroll
                for (int mi = 0; mi < 2; ++mi)
                    af[mi] = *reinterpret_cast<const bf16x8*>(sa + (32 * w + 16 * mi + r16) * 128 + coff);
#pragma unroll
                for (int ni = 0; ni < 7; ++ni)
                    bfv[ni] = *reinterpret_cast<const bf16x8*>(sb + (16 * ni + r16) * 128 + coff);
#pragma unroll
                for (int mi = 0; mi < 2; ++mi)
#pragma unroll
                    for (int ni = 0; ni < 7; ++ni)
                        acc[mi][ni] = __builtin_amdgcn_mfma_f32_16x16x32_bf16(bfv[ni], af[mi], acc[mi][ni], 0, 0, 0);
            }
            RAW_BARRIER();
        }
        STAMP(4, 2);
#pragma unroll
        for (int ni = 0; ni < 7; ++ni) {
            if (ni < 6 || full) {
                const float b0 = bf2f((bf16_t)(bb[ni][0] & 0xffffu)), b1 = bf2f((bf16_t)(bb[ni][0] >> 16));
                const float b2 = bf2f((bf16_t)(bb[ni][1] & 0xffffu)), b3 = bf2f((bf16_t)(bb[ni][1] >> 16));
#pragma unroll
                for (int mi = 0; mi < 2; ++mi) {
                    u32x2 pk;
                    pk[0] = pack2bf(rbf(acc[mi][ni][0]) + b0, rbf(acc[mi][ni][1]) + b1);
                    pk[1] = pack2bf(rbf(acc[mi][ni][2]) + b2, rbf(acc[mi][ni][3]) + b3);
                    *reinterpret_cast<u32x2*>(shm + (32 * w + 16 * mi + r16) * SSTR + (16 * ni + 4 * q4) * 2) = pk;
                }
            }
        }
    }
    RAW_BARRIER();
    const int cpr = width >> 3;
    for (int u = tid; u < TM * cpr; u += 640) {
        const int row = u / cpr, ch = u - row * cpr;
        const u32x4 v = *reinterpret_cast<const u32x4*>(shm + row * SSTR + 16 * ch);
        u32x4 o;
#pragma unroll
        for (int q = 0; q < 4; ++q)
            o[q] = pack2bf(gelu_tanh_f(bf2f((bf16_t)(v[q] & 0xffffu))), gelu_tanh_f(bf2f((bf16_t)(v[q] >> 16))));
        *reinterpret_cast<u32x4*>(C + (size_t)(m0 + row) * kF + n0 + 8 * ch) = o;
    }
    STAMP(4, 3);
}

extern "C" void kernel_launch(void* const* d_in, const int* in_sizes, int n_in,
                              void* d_out, int out_size, void* d_ws, size_t ws_size,
                              hipStream_t stream) {
    (void)in_sizes; (void)n_in; (void)out_size; (void)ws_size;
    const bf16_t* A_x  = (const bf16_t*)d_in[0];
    const bf16_t* A_wr = (const bf16_t*)d_in[1];
    const bf16_t* A_wi = (const bf16_t*)d_in[2];
    const bf16_t* B_w1 = (const bf16_t*)d_in[4];
    const bf16_t* B_b1 = (const bf16_t*)d_in[5];
    const bf16_t* B_w2 = (const bf16_t*)d_in[6];
    const bf16_t* B_b2 = (const bf16_t*)d_in[7];
    bf16_t* out = (bf16_t*)d_out;

    char* ws = (char*)d_ws;
    float2* T      = (float2*)(ws + OFF_T);
    float2* T2     = (float2*)(ws + OFF_T2);
    bf16_t* hbuf   = (bf16_t*)(ws + OFF_H);
    bf16_t* yln2   = (bf16_t*)(ws + OFF_YLN2);
    bf16_t* Hbuf   = (bf16_t*)(ws + OFF_G);
    bf16_t* W1t    = (bf16_t*)(ws + OFF_W1T);
    bf16_t* W2t    = (bf16_t*)(ws + OFF_W2T);
    float2* tw2048 = (float2*)(ws + OFF_TW2048);
    float2* tw640  = (float2*)(ws + OFF_TW640);
    unsigned* WT   = (unsigned*)(ws + OFF_WT);

    ln_rfft_kernel<<<kNBlkA, 512, 0, stream>>>(A_x, T, B_w1, B_w2, W1t, W2t, A_wr, A_wi, WT, tw2048, tw640);
    colfft_kernel<<<kDF, 256, 0, stream>>>(T, T2, WT, tw2048);
    irfft_ln_kernel<<<kS / kRowsWG, 512, 0, stream>>>(A_x, T2, tw640, hbuf, yln2);
    gemm1_kernel<<<256, 640, 0, stream>>>(yln2, W1t, B_b1, Hbuf);
    gemm2_kernel<<<256, 512, 0, stream>>>(Hbuf, W2t, B_b2, hbuf, out);
}
```

```cpp
#include <hip/hip_runtime.h>
#include <hip/hip_bf16.h>
#include <stdint.h>

static constexpr int kS  = 2048;
static constexpr int kD  = 640;
static constexpr int kDF = 321;
static constexpr int kF  = 1728;
static constexpr float kLnEps = 1e-5f;

static constexpr size_t OFF_T     = 0;
static constexpr size_t OFF_T2    = OFF_T   + (size_t)kDF * kS * 8;
static constexpr size_t OFF_H     = OFF_T2  + (size_t)kDF * kS * 8;
static constexpr size_t OFF_YLN2  = OFF_H   + (size_t)kS * kD * 2;
static constexpr size_t OFF_G     = OFF_YLN2+ (size_t)kS * kD * 2;
static constexpr size_t OFF_W1T   = OFF_G   + (size_t)kS * kF * 2;
static constexpr size_t OFF_W2T   = OFF_W1T + (size_t)kF * kD * 2;
static constexpr size_t OFF_TW2048= OFF_W2T + (size_t)kD * kF * 2;
static constexpr size_t OFF_TW640 = OFF_TW2048 + (size_t)2048 * 8;
static constexpr size_t OFF_WT    = OFF_TW640 + (size_t)640 * 8;

typedef unsigned short bf16_t;


__device__ __forceinline__ float bf2f(bf16_t u) {
    return __uint_as_float(((unsigned)u) << 16);
}
__device__ __forceinline__ bf16_t f2bf(float f) {
    return __builtin_bit_cast(bf16_t, (__bf16)f);
}
typedef __attribute__((ext_vector_type(2))) __bf16 bf16x2_t;
typedef __attribute__((ext_vector_type(2))) float f32x2_t;
__device__ __forceinline__ unsigned pack2bf(float lo, float hi) {
    f32x2_t v = {lo, hi};
    bf16x2_t b = __builtin_convertvector(v, bf16x2_t);
    return __builtin_bit_cast(unsigned, b);
}
__device__ __forceinline__ float rbf(float f) { return bf2f(f2bf(f)); }

__device__ __forceinline__ float2 cadd(float2 a, float2 b) { return make_float2(a.x + b.x, a.y + b.y); }
__device__ __forceinline__ float2 csub(float2 a, float2 b) { return make_float2(a.x - b.x, a.y - b.y); }
__device__ __forceinline__ float2 cmul(float2 a, float2 b) {
    return make_float2(__fmaf_rn(a.x, b.x, -a.y * b.y), __fmaf_rn(a.x, b.y, a.y * b.x));
}
__device__ __forceinline__ float2 cmulc(float2 a, float2 b) {
    return make_float2(__fmaf_rn(a.x, b.x, a.y * b.y), __fmaf_rn(a.y, b.x, -a.x * b.y));
}
template <bool INV>
__device__ __forceinline__ float2 cmul_j(float2 a) {
    return INV ? make_float2(-a.y, a.x) : make_float2(a.y, -a.x);
}

template <bool INV>
__device__ __forceinline__ void dft4(float2 &b0, float2 &b1, float2 &b2, float2 &b3) {
    float2 t0 = cadd(b0, b2), t1 = csub(b0, b2), t2 = cadd(b1, b3), t3 = cmul_j<INV>(csub(b1, b3));
    b0 = cadd(t0, t2); b2 = csub(t0, t2); b1 = cadd(t1, t3); b3 = csub(t1, t3);
}
template <bool INV>
__device__ __forceinline__ void dft5(float2 a[5]) {
    const float c1 = 0.30901699437494745f, c2 = -0.8090169943749473f;
    const float s1 = 0.9510565162951535f,  s2 = 0.5877852522924731f;
    float2 b1 = cadd(a[1], a[4]), b4 = csub(a[1], a[4]);
    float2 b2 = cadd(a[2], a[3]), b3 = csub(a[2], a[3]);
    float2 t1 = make_float2(a[0].x + c1 * b1.x + c2 * b2.x, a[0].y + c1 * b1.y + c2 * b2.y);
    float2 t2 = make_float2(a[0].x + c2 * b1.x + c1 * b2.x, a[0].y + c2 * b1.y + c1 * b2.y);
    float2 u1 = make_float2(s1 * b4.x + s2 * b3.x, s1 * b4.y + s2 * b3.y);
    float2 u2 = make_float2(s2 * b4.x - s1 * b3.x, s2 * b4.y - s1 * b3.y);
    float2 iu1 = cmul_j<INV>(u1), iu2 = cmul_j<INV>(u2);
    a[0] = make_float2(a[0].x + b1.x + b2.x, a[0].y + b1.y + b2.y);
    a[1] = cadd(t1, iu1); a[4] = csub(t1, iu1);
    a[2] = cadd(t2, iu2); a[3] = csub(t2, iu2);
}
template <bool INV>
__device__ __forceinline__ void dft8(float2 a[8]) {
    const float r2 = 0.70710678118654752f;
    float2 e0 = a[0], e1 = a[2], e2 = a[4], e3 = a[6];
    float2 o0 = a[1], o1 = a[3], o2 = a[5], o3 = a[7];
    dft4<INV>(e0, e1, e2, e3);
    dft4<INV>(o0, o1, o2, o3);
    float2 p1, p3;
    if (!INV) {
        p1 = make_float2((o1.x + o1.y) * r2, (o1.y - o1.x) * r2);
        p3 = make_float2((o3.y - o3.x) * r2, -(o3.x + o3.y) * r2);
    } else {
        p1 = make_float2((o1.x - o1.y) * r2, (o1.x + o1.y) * r2);
        p3 = make_float2(-(o3.x + o3.y) * r2, (o3.x - o3.y) * r2);
    }
    float2 p2 = cmul_j<INV>(o2);
    a[0] = cadd(e0, o0); a[4] = csub(e0, o0);
    a[1] = cadd(e1, p1); a[5] = csub(e1, p1);
    a[2] = cadd(e2, p2); a[6] = csub(e2, p2);
    a[3] = cadd(e3, p3); a[7] = csub(e3, p3);
}

template <int R, bool INV> struct Dft;
template <bool INV> struct Dft<4, INV> {
    __device__ __forceinline__ static void run(float2 a[4]) { dft4<INV>(a[0], a[1], a[2], a[3]); }
};
template <bool INV> struct Dft<5, INV> {
    __device__ __forceinline__ static void run(float2 a[5]) { dft5<INV>(a); }
};
template <bool INV> struct Dft<8, INV> {
    __device__ __forceinline__ static void run(float2 a[8]) { dft8<INV>(a); }
};

template <int R, int NT, int NN, int SS, int NROWS, int TWSCALE, bool INV>
__device__ __forceinline__ void stockham_pass(const float2* __restrict__ in, float2* __restrict__ out,
                                              const float2* __restrict__ tw, int tid, int nthreads) {
    constexpr int M = NN / R;
    constexpr int NB = NT / R;
    for (int bt = tid; bt < NROWS * NB; bt += nthreads) {
        const int r   = bt / NB;
        const int rem = bt - r * NB;
        const int p   = rem / SS;
        const int q   = rem - p * SS;
        const float2* rin = in + r * NT;
        float2* rout = out + r * NT;
        float2 a[R];
#pragma unroll
        for (int k = 0; k < R; ++k) a[k] = rin[SS * (p + k * M) + q];
        Dft<R, INV>::run(a);
        if (NN != R) {
#pragma unroll
            for (int k = 1; k < R; ++k) {
                float2 w = tw[p * k * TWSCALE];
                a[k] = INV ? cmulc(a[k], w) : cmul(a[k], w);
            }
        }
#pragma unroll
        for (int k = 0; k < R; ++k) rout[SS * (R * p + k) + q] = a[k];
    }
}

static constexpr int kRowsWG = 8;
static constexpr int kNBlkFFT = kS / kRowsWG;
static constexpr int kNT1 = (kD / 64) * (kF / 64);
static constexpr int kNT2 = (kF / 64) * (kD / 64);
static constexpr int kNTW = (kS / 64) * ((kDF + 63) / 64);
static constexpr int kNTB = (2048 + 640 + 255) / 256;
static constexpr int kNBlkA = kNBlkFFT + kNT1 + kNT2 + kNTW + kNTB;

__device__ __forceinline__ void transpose_tile64(const bf16_t* __restrict__ src, bf16_t* __restrict__ dst,
                                                 int R, int C, int r0, int c0, bf16_t (*tile)[66], int tid) {
#pragma unroll
    for (int i = 0; i < 2; ++i) {
        int u = tid + 256 * i;
        int row = u >> 3, ch = u & 7;
        uint4 v = *reinterpret_cast<const uint4*>(src + (size_t)(r0 + row) * C + c0 + 8 * ch);
        const bf16_t* e = reinterpret_cast<const bf16_t*>(&v);
#pragma unroll
        for (int j = 0; j < 8; ++j) tile[row][8 * ch + j] = e[j];
    }
    __syncthreads();
#pragma unroll
    for (int i = 0; i < 2; ++i) {
        int u = tid + 256 * i;
        int orow = u >> 3, ch = u & 7;
        uint4 v;
        bf16_t* e = reinterpret_cast<bf16_t*>(&v);
#pragma unroll
        for (int j = 0; j < 8; ++j) e[j] = tile[8 * ch + j][orow];
        *reinterpret_cast<uint4*>(dst + (size_t)(c0 + orow) * R + r0 + 8 * ch) = v;
    }
}

__global__ void __launch_bounds__(512) ln_rfft_kernel(const bf16_t* __restrict__ x,
                                                      float2* __restrict__ T,
                                                      const bf16_t* __restrict__ W1, const bf16_t* __restrict__ W2,
                                                      bf16_t* __restrict__ W1t, bf16_t* __restrict__ W2t,
                                                      const bf16_t* __restrict__ w_r, const bf16_t* __restrict__ w_i,
                                                      unsigned* __restrict__ WT,
                                                      float2* __restrict__ tw2048_g, float2* __restrict__ tw640_g) {
    __shared__ float2 tw[640];
    __shared__ __attribute__((aligned(16))) float2 buf[2][kRowsWG * 320];
    const int tid = threadIdx.x;
    const int bid = blockIdx.x;
    if (bid >= kNBlkFFT) {
        if (tid >= 256) return;
        int b = bid - kNBlkFFT;
        if (b < kNT1) {
            bf16_t (*tile)[66] = reinterpret_cast<bf16_t (*)[66]>(&buf[0][0]);
            transpose_tile64(W1, W1t, kD, kF, (b / (kF / 64)) * 64, (b % (kF / 64)) * 64, tile, tid);
            return;
        }
        b -= kNT1;
        if (b < kNT2) {
            bf16_t (*tile)[66] = reinterpret_cast<bf16_t (*)[66]>(&buf[0][0]);
            transpose_tile64(W2, W2t, kF, kD, (b / (kD / 64)) * 64, (b % (kD / 64)) * 64, tile, tid);
            return;
        }
        b -= kNT2;
        if (b < kNTW) {
            unsigned (*tile)[65] = reinterpret_cast<unsigned (*)[65]>(&buf[0][0]);
            const int s0 = (b / 6) * 64, l0 = (b % 6) * 64;
            const int ll = tid & 63, sr = tid >> 6;
            if (l0 + ll < kDF) {
#pragma unroll
                for (int i = 0; i < 16; ++i) {
                    const int s = sr + 4 * i;
                    const size_t idx = (size_t)(s0 + s) * kDF + l0 + ll;
                    tile[s][ll] = (unsigned)w_r[idx] | ((unsigned)w_i[idx] << 16);
                }
            }
            __syncthreads();
            const int ss = tid & 63, lr = tid >> 6;
#pragma unroll
            for (int i = 0; i < 16; ++i) {
                const int l = lr + 4 * i;
                if (l0 + l < kDF) WT[(size_t)(l0 + l) * kS + s0 + ss] = tile[ss][l];
            }
            return;
        }
        b -= kNTW;
        {
            const int idx = b * 256 + tid;
            if (idx < 2048) {
                float s, c;
                sincospif(-2.0f * (float)idx / 2048.0f, &s, &c);
                tw2048_g[idx] = make_float2(c, s);
            } else if (idx < 2048 + 640) {
                float s, c;
                sincospif(-2.0f * (float)(idx - 2048) / 640.0f, &s, &c);
                tw640_g[idx - 2048] = make_float2(c, s);
            }
            return;
        }
    }
    const int row0 = bid * kRowsWG;
    const int r = tid >> 6, l64 = tid & 63;
    const bf16_t* xr = x + (size_t)(row0 + r) * kD;
    unsigned xv[5];
#pragma unroll
    for (int i = 0; i < 5; ++i) xv[i] = *reinterpret_cast<const unsigned*>(xr + 2 * (l64 + 64 * i));
    for (int i = tid; i < 640; i += 512) {
        float sn, cs;
        sincospif(-2.0f * (float)i / 640.0f, &sn, &cs);
        tw[i] = make_float2(cs, sn);
    }
    float v[10];
    float sum = 0.f;
#pragma unroll
    for (int i = 0; i < 5; ++i) {
        v[2 * i + 0] = bf2f((bf16_t)(xv[i] & 0xffffu)); v[2 * i + 1] = bf2f((bf16_t)(xv[i] >> 16));
        sum += v[2 * i] + v[2 * i + 1];
    }
#pragma unroll
    for (int off = 32; off > 0; off >>= 1) sum += __shfl_xor(sum, off);
    const float mean = sum * (1.0f / kD);
    const float mb = rbf(mean);
    float ss = 0.f;
#pragma unroll
    for (int i = 0; i < 10; ++i) { float d = v[i] - mean; ss += d * d; }
#pragma unroll
    for (int off = 32; off > 0; off >>= 1) ss += __shfl_xor(ss, off);
    const float var = ss * (1.0f / kD);
    const float den = rbf(sqrtf(rbf(rbf(var) + kLnEps)));
#pragma unroll
    for (int i = 0; i < 5; ++i)
        buf[0][r * 320 + l64 + 64 * i] = make_float2(rbf(rbf(v[2 * i] - mb) / den), rbf(rbf(v[2 * i + 1] - mb) / den));
    __syncthreads();
    stockham_pass<8, 320, 320, 1, kRowsWG, 2, false>(buf[0], buf[1], tw, tid, 512);
    __syncthreads();
    stockham_pass<8, 320, 40, 8, kRowsWG, 16, false>(buf[1], buf[0], tw, tid, 512);
    __syncthreads();
    stockham_pass<5, 320, 5, 64, kRowsWG, 128, false>(buf[0], buf[1], tw, tid, 512);
    __syncthreads();
    const float2* Z = buf[1];
    for (int task = tid; task < 2 * kDF; task += 512) {
        const int k = task >> 1, rh = (task & 1) * 4;
        const int km = (320 - k) % 320;
        const int kk = (k == 320) ? 0 : k;
        const float2 w = tw[k];
        float2 outv[4];
#pragma unroll
        for (int rr = 0; rr < 4; ++rr) {
            const float2 Ze = Z[(rh + rr) * 320 + kk];
            const float2 Zm = Z[(rh + rr) * 320 + km];
            const float2 E = make_float2(0.5f * (Ze.x + Zm.x), 0.5f * (Ze.y - Zm.y));
            const float dx = Ze.x - Zm.x, dy = Ze.y + Zm.y;
            const float2 O = make_float2(0.5f * dy, -0.5f * dx);
            outv[rr] = cadd(E, cmul(w, O));
        }
        float4* dst = reinterpret_cast<float4*>(T + (size_t)k * kS + row0 + rh);
        dst[0] = make_float4(outv[0].x, outv[0].y, outv[1].x, outv[1].y);
        dst[1] = make_float4(outv[2].x, outv[2].y, outv[3].x, outv[3].y);
    }
}

__global__ void __launch_bounds__(256) colfft_kernel(const float2* __restrict__ T,
                                                     float2* __restrict__ T2,
                                                     const unsigned* __restrict__ WT,
                                                     const float2* __restrict__ tw2048_g) {
    __shared__ __attribute__((aligned(16))) float2 tw[2048];
    __shared__ __attribute__((aligned(16))) float2 buf[2][2048];
    const int tid = threadIdx.x;
    const int l = blockIdx.x;
    float2 a[8], w1[8];
    const float2* col = T + (size_t)l * kS;
#pragma unroll
    for (int k = 0; k < 8; ++k) a[k] = col[tid + 256 * k];
#pragma unroll
    for (int k = 1; k < 8; ++k) w1[k] = tw2048_g[tid * k];
    unsigned wp[8];
#pragma unroll
    for (int j = 0; j < 8; ++j) wp[j] = WT[(size_t)l * kS + tid + 256 * j];
    const float4* tw4 = reinterpret_cast<const float4*>(tw2048_g);
    float4* twd = reinterpret_cast<float4*>(tw);
    float4 tv[4];
#pragma unroll
    for (int i = 0; i < 4; ++i) tv[i] = tw4[tid + 256 * i];
    dft8<false>(a);
#pragma unroll
    for (int k = 1; k < 8; ++k) a[k] = cmul(a[k], w1[k]);
#pragma unroll
    for (int k = 0; k < 8; ++k) buf[1][8 * tid + k] = a[k];
#pragma unroll
    for (int i = 0; i < 4; ++i) twd[tid + 256 * i] = tv[i];
    (void)buf[0][0];
    __syncthreads();
    stockham_pass<8, 2048, 256, 8, 1, 8, false>(buf[1], buf[0], tw, tid, 256);
    __syncthreads();
    stockham_pass<8, 2048, 32, 64, 1, 64, false>(buf[0], buf[1], tw, tid, 256);
    __syncthreads();
    {
        const float sc = 1.0f / 2048.0f;
#pragma unroll
        for (int t2 = 0; t2 < 2; ++t2) {
            const int q = tid + 256 * t2;
            float2 a[4];
#pragma unroll
            for (int k = 0; k < 4; ++k) a[k] = buf[1][512 * k + q];
            dft4<false>(a[0], a[1], a[2], a[3]);
#pragma unroll
            for (int k = 0; k < 4; ++k) {
                const int j = 2 * k + t2;
                const float2 wc = make_float2(bf2f((bf16_t)(wp[j] & 0xffffu)) * sc, bf2f((bf16_t)(wp[j] >> 16)) * sc);
                buf[0][512 * k + q] = cmul(a[k], wc);
            }
        }
    }
    __syncthreads();
    stockham_pass<8, 2048, 2048, 1, 1, 1, true>(buf[0], buf[1], tw, tid, 256);
    __syncthreads();
    stockham_pass<8, 2048, 256, 8, 1, 8, true>(buf[1], buf[0], tw, tid, 256);
    __syncthreads();
    stockham_pass<8, 2048, 32, 64, 1, 64, true>(buf[0], buf[1], tw, tid, 256);
    __syncthreads();
#pragma unroll
    for (int t2 = 0; t2 < 2; ++t2) {
        const int q = tid + 256 * t2;
        float2 a[4];
#pragma unroll
        for (int k = 0; k < 4; ++k) a[k] = buf[1][512 * k + q];
        dft4<true>(a[0], a[1], a[2], a[3]);
#pragma unroll
        for (int k = 0; k < 4; ++k) T2[(size_t)l * kS + 512 * k + q] = a[k];
    }
}

__global__ void __launch_bounds__(512) irfft_ln_kernel(const bf16_t* __restrict__ x,
                                                       const float2* __restrict__ T2,
                                                       const float2* __restrict__ tw640_g,
                                                       bf16_t* __restrict__ hout,
                                                       bf16_t* __restrict__ yln2) {
    __shared__ float2 tw[640];
    __shared__ __attribute__((aligned(16))) float2 X[kRowsWG][322];
    __shared__ __attribute__((aligned(16))) float2 buf[2][kRowsWG * 320];
    const int tid = threadIdx.x;
    const int row0 = (((blockIdx.x & 7) * 32) + (blockIdx.x >> 3)) * kRowsWG;
    const int r = tid >> 6, l64 = tid & 63;
    const bf16_t* xrow = x + (size_t)(row0 + r) * kD;
    unsigned xv[5];
#pragma unroll
    for (int i = 0; i < 5; ++i) xv[i] = *reinterpret_cast<const unsigned*>(xrow + 2 * (l64 + 64 * i));
    for (int i = tid; i < 640; i += 512) tw[i] = tw640_g[i];
    for (int task = tid; task < 2 * kDF; task += 512) {
        const int k = task >> 1, rh = (task & 1) * 4;
        const float4* src = reinterpret_cast<const float4*>(T2 + (size_t)k * kS + row0 + rh);
        const float4 v0 = src[0], v1 = src[1];
        X[rh + 0][k] = make_float2(v0.x, v0.y); X[rh + 1][k] = make_float2(v0.z, v0.w);
        X[rh + 2][k] = make_float2(v1.x, v1.y); X[rh + 3][k] = make_float2(v1.z, v1.w);
    }
    __syncthreads();
    if (tid < kRowsWG * 40) {
        const int rr = tid / 40, p = tid - rr * 40;
        float2 a[8];
#pragma unroll
        for (int k = 0; k < 8; ++k) {
            const int j = p + 40 * k;
            float2 Xk = X[rr][j];
            float2 Xm = X[rr][320 - j];
            if (j == 0) { Xk.y = 0.f; Xm.y = 0.f; }
            const float2 E = make_float2(0.5f * (Xk.x + Xm.x), 0.5f * (Xk.y - Xm.y));
            const float2 d = make_float2(Xk.x - Xm.x, Xk.y + Xm.y);
            float2 O = cmulc(d, tw[j]);
            O.x *= 0.5f; O.y *= 0.5f;
            a[k] = make_float2(E.x - O.y, E.y + O.x);
        }
        dft8<true>(a);
#pragma unroll
        for (int k = 1; k < 8; ++k) a[k] = cmulc(a[k], tw[2 * p * k]);
        float2* rout = buf[1] + rr * 320;
#pragma unroll
        for (int k = 0; k < 8; ++k) rout[8 * p + k] = a[k];
    }
    __syncthreads();
    stockham_pass<8, 320, 40, 8, kRowsWG, 16, true>(buf[1], buf[0], tw, tid, 512);
    __syncthreads();
    stockham_pass<5, 320, 5, 64, kRowsWG, 128, true>(buf[0], buf[1], tw, tid, 512);
    __syncthreads();
    const float2* zr = buf[1] + r * 320;
    bf16_t* hrow = hout + (size_t)(row0 + r) * kD;
    bf16_t* yrow = yln2 + (size_t)(row0 + r) * kD;
    float hv[10];
    float sum = 0.f;
#pragma unroll
    for (int i = 0; i < 5; ++i) {
        const int c = l64 + 64 * i;
        const float2 zv = zr[c];
        const float h0 = rbf(bf2f((bf16_t)(xv[i] & 0xffffu)) + rbf(zv.x * (1.0f / 320.0f)));
        const float h1 = rbf(bf2f((bf16_t)(xv[i] >> 16)) + rbf(zv.y * (1.0f / 320.0f)));
        hv[2 * i] = h0; hv[2 * i + 1] = h1;
        *reinterpret_cast<unsigned*>(hrow + 2 * c) = pack2bf(h0, h1);
        sum += h0 + h1;
    }
#pragma unroll
    for (int off = 32; off > 0; off >>= 1) sum += __shfl_xor(sum, off);
    const float mean = sum * (1.0f / kD);
    const float mb = rbf(mean);
    float ss = 0.f;
#pragma unroll
    for (int i = 0; i < 10; ++i) { float d = hv[i] - mean; ss += d * d; }
#pragma unroll
    for (int off = 32; off > 0; off >>= 1) ss += __shfl_xor(ss, off);
    const float var = ss * (1.0f / kD);
    const float den = rbf(sqrtf(rbf(rbf(var) + kLnEps)));
    float y1[10];
    float sum2 = 0.f;
#pragma unroll
    for (int i = 0; i < 10; ++i) { y1[i] = rbf(rbf(hv[i] - mb) / den); sum2 += y1[i]; }
#pragma unroll
    for (int off = 32; off > 0; off >>= 1) sum2 += __shfl_xor(sum2, off);
    const float mean2 = sum2 * (1.0f / kD);
    const float mb2 = rbf(mean2);
    float ss2 = 0.f;
#pragma unroll
    for (int i = 0; i < 10; ++i) { float d = y1[i] - mean2; ss2 += d * d; }
#pragma unroll
    for (int off = 32; off > 0; off >>= 1) ss2 += __shfl_xor(ss2, off);
    const float var2 = ss2 * (1.0f / kD);
    const float den2 = rbf(sqrtf(rbf(rbf(var2) + kLnEps)));
#pragma unroll
    for (int i = 0; i < 5; ++i) {
        const int c = l64 + 64 * i;
        *reinterpret_cast<unsigned*>(yrow + 2 * c) = pack2bf(rbf(y1[2 * i] - mb2) / den2, rbf(y1[2 * i + 1] - mb2) / den2);
    }
}

using bf16x8 = __attribute__((ext_vector_type(8))) __bf16;
using f32x16 = __attribute__((ext_vector_type(16))) float;
typedef unsigned __attribute__((ext_vector_type(4))) u32x4;
typedef unsigned __attribute__((ext_vector_type(2))) u32x2;

#define WAIT_V(n) asm volatile("s_waitcnt vmcnt(%0)" ::"n"(n) : "memory")
#define WAIT_L(n) asm volatile("s_waitcnt lgkmcnt(%0)" ::"n"(n) : "memory")
#define RAW_BARRIER() do { WAIT_L(0); __builtin_amdgcn_s_barrier(); } while (0)

__device__ __forceinline__ float gelu_tanh_f(float t) {
    const float x2 = t * __fmaf_rn(0.0713548162726008f, t * t, 1.5957691216057308f);
    return t * __frcp_rn(1.0f + __expf(-x2));
}

using f32x4v = __attribute__((ext_vector_type(4))) float;

__global__ void __launch_bounds__(512) gemm2_kernel(const bf16_t* __restrict__ A, const bf16_t* __restrict__ Bt,
                                                    const bf16_t* __restrict__ bias,
                                                    const bf16_t* __restrict__ resid,
                                                    bf16_t* __restrict__ C) {
    constexpr int TM = 64, TN = 80, BK = 64, KT = kF / BK;
    constexpr int NBUF = 6, D = NBUF - 1;
    constexpr int STAGE_B = (TM + TN) * 128;
    constexpr int GLW = 3;
    constexpr int SSTR = TN * 2 + 16;
    static_assert((D - 1) * GLW <= 63, "vmcnt range");
    static_assert(TM * SSTR <= NBUF * STAGE_B, "staging fits");
    __shared__ __attribute__((aligned(1024))) char shm[NBUF * STAGE_B];

    const int tid = threadIdx.x, lane = tid & 63, wave8 = tid >> 6;
    const int xcd = blockIdx.x & 7, idx = blockIdx.x >> 3;
    const int m0 = (xcd * 4 + idx / 8) * TM, n0 = (idx % 8) * TN;
    if (wave8 >= 2) {
        const int lw = wave8 - 2;
        int goff[GLW];
#pragma unroll
        for (int q = 0; q < GLW; ++q) {
            const int blk = lw * GLW + q;
            const int rowl = 8 * (blk < 8 ? blk : blk - 8) + (lane >> 3);
            const int c = (lane & 7) ^ ((rowl >> 1) & 7);
            goff[q] = (blk < 8 ? (m0 + rowl) : (n0 + rowl)) * kF + 8 * c;
        }
#define GLDS2(BUF, KTILE)                                                                            \
    do {                                                                                             \
        char* sa_ = shm + (BUF) * STAGE_B;                                                           \
        _Pragma("unroll") for (int q_ = 0; q_ < GLW; ++q_) {                                         \
            const int blk_ = lw * GLW + q_;                                                          \
            const bf16_t* src_ = (blk_ < 8 ? A : Bt) + goff[q_] + (KTILE) * BK;                      \
            __builtin_amdgcn_global_load_lds((const unsigned*)src_, (unsigned*)(sa_ + blk_ * 1024), 16, 0, 0); \
        }                                                                                            \
    } while (0)
#pragma unroll
        for (int pt = 0; pt < D; ++pt) GLDS2(pt, pt);
        WAIT_V((D - 1) * GLW);
        RAW_BARRIER();
        for (int kt = 0; kt < KT; ++kt) {
            if (kt + D < KT) {
                GLDS2((kt + D) % NBUF, kt + D);
                WAIT_V((D - 1) * GLW);
            } else {
                WAIT_V(0);
            }
            RAW_BARRIER();
        }
#undef GLDS2
    } else {
        const int w = wave8;
        const int r16 = lane & 15, q4 = lane >> 4;
        const int sw = (r16 >> 1) & 7;
        f32x4v acc[2][5];
#pragma unroll
        for (int i = 0; i < 2; ++i)
#pragma unroll
            for (int j = 0; j < 5; ++j) acc[i][j] = (f32x4v){0.f, 0.f, 0.f, 0.f};
        u32x2 bb[5];
#pragma unroll
        for (int ni = 0; ni < 5; ++ni) bb[ni] = *reinterpret_cast<const u32x2*>(bias + n0 + 16 * ni + 4 * q4);
        RAW_BARRIER();
        for (int kt = 0; kt < KT; ++kt) {
            const char* sa = shm + (kt % NBUF) * STAGE_B;
            const char* sb = sa + TM * 128;
            bf16x8 af[2][2], bfv[2][5];
#pragma unroll
            for (int ks = 0; ks < 2; ++ks) {
                const int coff = 16 * ((4 * ks + q4) ^ sw);
#pragma unroll
                for (int mi = 0; mi < 2; ++mi)
                    af[ks][mi] = *reinterpret_cast<const bf16x8*>(sa + (32 * w + 16 * mi + r16) * 128 + coff);
#pragma unroll
                for (int ni = 0; ni < 5; ++ni)
                    bfv[ks][ni] = *reinterpret_cast<const bf16x8*>(sb + (16 * ni + r16) * 128 + coff);
            }
#pragma unroll
            for (int ks = 0; ks < 2; ++ks)
#pragma unroll
                for (int mi = 0; mi < 2; ++mi)
#pragma unroll
                    for (int ni = 0; ni < 5; ++ni)
                        acc[mi][ni] = __builtin_amdgcn_mfma_f32_16x16x32_bf16(bfv[ks][ni], af[ks][mi], acc[mi][ni], 0, 0, 0);
            RAW_BARRIER();
        }
#pragma unroll
        for (int ni = 0; ni < 5; ++ni) {
            const float b0 = bf2f((bf16_t)(bb[ni][0] & 0xffffu)), b1 = bf2f((bf16_t)(bb[ni][0] >> 16));
            const float b2 = bf2f((bf16_t)(bb[ni][1] & 0xffffu)), b3 = bf2f((bf16_t)(bb[ni][1] >> 16));
#pragma unroll
            for (int mi = 0; mi < 2; ++mi) {
                u32x2 pk;
                pk[0] = pack2bf(rbf(acc[mi][ni][0]) + b0, rbf(acc[mi][ni][1]) + b1);
                pk[1] = pack2bf(rbf(acc[mi][ni][2]) + b2, rbf(acc[mi][ni][3]) + b3);
                *reinterpret_cast<u32x2*>(shm + (32 * w + 16 * mi + r16) * SSTR + (16 * ni + 4 * q4) * 2) = pk;
            }
        }
    }
    RAW_BARRIER();
    constexpr int CPR = TN / 8;
    for (int u = tid; u < TM * CPR; u += 512) {
        const int row = u / CPR, ch = u % CPR;
        const u32x4 v = *reinterpret_cast<const u32x4*>(shm + row * SSTR + 16 * ch);
        const size_t go = (size_t)(m0 + row) * kD + n0 + 8 * ch;
        const u32x4 hr = *reinterpret_cast<const u32x4*>(resid + go);
        u32x4 o;
#pragma unroll
        for (int q = 0; q < 4; ++q) {
            const float lo = bf2f((bf16_t)(hr[q] & 0xffffu)) + bf2f((bf16_t)(v[q] & 0xffffu));
            const float hi = bf2f((bf16_t)(hr[q] >> 16)) + bf2f((bf16_t)(v[q] >> 16));
            o[q] = pack2bf(lo, hi);
        }
        *reinterpret_cast<u32x4*>(C + go) = o;
    }
}

__global__ void __launch_bounds__(640) gemm1_kernel(const bf16_t* __restrict__ A, const bf16_t* __restrict__ Bt,
                                                    const bf16_t* __restrict__ bias,
                                                    bf16_t* __restrict__ C) {
    constexpr int TM = 128, TNMAX = 112, BK = 64, KT = kD / BK;
    constexpr int NBUF = 5, D = NBUF - 1;
    constexpr int STAGE_B = (TM + TNMAX) * 128;
    constexpr int GLW = 5;
    constexpr int SSTR = TNMAX * 2 + 16;
    static_assert((D - 1) * GLW <= 63, "vmcnt range");
    static_assert(TM * SSTR <= NBUF * STAGE_B, "staging fits");
    __shared__ __attribute__((aligned(1024))) char shm[NBUF * STAGE_B];

    const int tid = threadIdx.x, lane = tid & 63, wave10 = tid >> 6;
    const int xcd = blockIdx.x & 7, idx = blockIdx.x >> 3;
    const int j = idx & 15;
    const int m0 = (xcd * 2 + (idx >> 4)) * TM;
    const int width = (j < 12) ? 112 : 96;
    const int n0 = (j < 12) ? 112 * j : 1344 + 96 * (j - 12);
    if (wave10 >= 4) {
        const int lw = wave10 - 4;
        int goff[GLW];
#pragma unroll
        for (int q = 0; q < GLW; ++q) {
            const int blk = lw * GLW + q;
            const int rowl = 8 * (blk < 16 ? blk : blk - 16) + (lane >> 3);
            const int c = (lane & 7) ^ ((rowl >> 1) & 7);
            const int grow = (blk < 16) ? (m0 + rowl) : (n0 + (rowl < width ? rowl : width - 1));
            goff[q] = grow * kD + 8 * c;
        }
#define GLDS1(BUF, KTILE)                                                                            \
    do {                                                                                             \
        char* sa_ = shm + (BUF) * STAGE_B;                                                           \
        _Pragma("unroll") for (int q_ = 0; q_ < GLW; ++q_) {                                         \
            const int blk_ = lw * GLW + q_;                                                          \
            const bf16_t* src_ = (blk_ < 16 ? A : Bt) + goff[q_] + (KTILE) * BK;                     \
            __builtin_amdgcn_global_load_lds((const unsigned*)src_, (unsigned*)(sa_ + blk_ * 1024), 16, 0, 0); \
        }                                                                                            \
    } while (0)
#pragma unroll
        for (int pt = 0; pt < D; ++pt) GLDS1(pt, pt);
        WAIT_V((D - 1) * GLW);
        RAW_BARRIER();
        for (int kt = 0; kt < KT; ++kt) {
            if (kt + D < KT) {
                GLDS1((kt + D) % NBUF, kt + D);
                WAIT_V((D - 1) * GLW);
            } else {
                WAIT_V(0);
            }
            RAW_BARRIER();
        }
#undef GLDS1
    } else {
        const int w = wave10;
        const int r16 = lane & 15, q4 = lane >> 4;
        const int sw = (r16 >> 1) & 7;
        const bool full = (width == 112);
        f32x4v acc[2][7];
#pragma unroll
        for (int i = 0; i < 2; ++i)
#pragma unroll
            for (int jj = 0; jj < 7; ++jj) acc[i][jj] = (f32x4v){0.f, 0.f, 0.f, 0.f};
        u32x2 bb[7];
#pragma unroll
        for (int ni = 0; ni < 7; ++ni)
            bb[ni] = (ni < 6 || full) ? *reinterpret_cast<const u32x2*>(bias + n0 + 16 * ni + 4 * q4) : (u32x2){0u, 0u};
        RAW_BARRIER();
        for (int kt = 0; kt < KT; ++kt) {
            const char* sa = shm + (kt % NBUF) * STAGE_B;
            const char* sb = sa + TM * 128;
#pragma unroll
            for (int ks = 0; ks < 2; ++ks) {
                const int coff = 16 * ((4 * ks + q4) ^ sw);
                bf16x8 af[2], bfv[7];
#pragma unroll
                for (int mi = 0; mi < 2; ++mi)
                    af[mi] = *reinterpret_cast<const bf16x8*>(sa + (32 * w + 16 * mi + r16) * 128 + coff);
#pragma unroll
                for (int ni = 0; ni < 7; ++ni)
                    bfv[ni] = *reinterpret_cast<const bf16x8*>(sb + (16 * ni + r16) * 128 + coff);
#pragma unroll
                for (int mi = 0; mi < 2; ++mi)
#pragma unroll
                    for (int ni = 0; ni < 7; ++ni)
                        acc[mi][ni] = __builtin_amdgcn_mfma_f32_16x16x32_bf16(bfv[ni], af[mi], acc[mi][ni], 0, 0, 0);
            }
            RAW_BARRIER();
        }
#pragma unroll
        for (int ni = 0; ni < 7; ++ni) {
            if (ni < 6 || full) {
                const float b0 = bf2f((bf16_t)(bb[ni][0] & 0xffffu)), b1 = bf2f((bf16_t)(bb[ni][0] >> 16));
                const float b2 = bf2f((bf16_t)(bb[ni][1] & 0xffffu)), b3 = bf2f((bf16_t)(bb[ni][1] >> 16));
#pragma unroll
                for (int mi = 0; mi < 2; ++mi) {
                    u32x2 pk;
                    pk[0] = pack2bf(rbf(acc[mi][ni][0]) + b0, rbf(acc[mi][ni][1]) + b1);
                    pk[1] = pack2bf(rbf(acc[mi][ni][2]) + b2, rbf(acc[mi][ni][3]) + b3);
                    *reinterpret_cast<u32x2*>(shm + (32 * w + 16 * mi + r16) * SSTR + (16 * ni + 4 * q4) * 2) = pk;
                }
            }
        }
    }
    RAW_BARRIER();
    const int cpr = width >> 3;
    for (int u = tid; u < TM * cpr; u += 640) {
        const int row = u / cpr, ch = u - row * cpr;
        const u32x4 v = *reinterpret_cast<const u32x4*>(shm + row * SSTR + 16 * ch);
        u32x4 o;
#pragma unroll
        for (int q = 0; q < 4; ++q)
            o[q] = pack2bf(gelu_tanh_f(bf2f((bf16_t)(v[q] & 0xffffu))), gelu_tanh_f(bf2f((bf16_t)(v[q] >> 16))));
        *reinterpret_cast<u32x4*>(C + (size_t)(m0 + row) * kF + n0 + 8 * ch) = o;
    }
}

extern "C" void kernel_launch(void* const* d_in, const int* in_sizes, int n_in,
                              void* d_out, int out_size, void* d_ws, size_t ws_size,
                              hipStream_t stream) {
    (void)in_sizes; (void)n_in; (void)out_size; (void)ws_size;
    const bf16_t* A_x  = (const bf16_t*)d_in[0];
    const bf16_t* A_wr = (const bf16_t*)d_in[1];
    const bf16_t* A_wi = (const bf16_t*)d_in[2];
    const bf16_t* B_w1 = (const bf16_t*)d_in[4];
    const bf16_t* B_b1 = (const bf16_t*)d_in[5];
    const bf16_t* B_w2 = (const bf16_t*)d_in[6];
    const bf16_t* B_b2 = (const bf16_t*)d_in[7];
    bf16_t* out = (bf16_t*)d_out;

    char* ws = (char*)d_ws;
    float2* T      = (float2*)(ws + OFF_T);
    float2* T2     = (float2*)(ws + OFF_T2);
    bf16_t* hbuf   = (bf16_t*)(ws + OFF_H);
    bf16_t* yln2   = (bf16_t*)(ws + OFF_YLN2);
    bf16_t* Hbuf   = (bf16_t*)(ws + OFF_G);
    bf16_t* W1t    = (bf16_t*)(ws + OFF_W1T);
    bf16_t* W2t    = (bf16_t*)(ws + OFF_W2T);
    float2* tw2048 = (float2*)(ws + OFF_TW2048);
    float2* tw640  = (float2*)(ws + OFF_TW640);
    unsigned* WT   = (unsigned*)(ws + OFF_WT);

    ln_rfft_kernel<<<kNBlkA, 512, 0, stream>>>(A_x, T, B_w1, B_w2, W1t, W2t, A_wr, A_wi, WT, tw2048, tw640);
    colfft_kernel<<<kDF, 256, 0, stream>>>(T, T2, WT, tw2048);
    irfft_ln_kernel<<<kS / kRowsWG, 512, 0, stream>>>(A_x, T2, tw640, hbuf, yln2);
    gemm1_kernel<<<256, 640, 0, stream>>>(yln2, W1t, B_b1, Hbuf);
    gemm2_kernel<<<256, 512, 0, stream>>>(Hbuf, W2t, B_b2, hbuf, out);
}
```
